```python
import torch

def _softmax(x, axis=-1):
    return torch.softmax(x, dim=axis)

def _gelu(x):
    return torch.nn.functional.gelu(x, approximate='tanh')

def _silu(x):
    return torch.nn.functional.silu(x)

def _sigmoid(x):
    return torch.sigmoid(x)

def _layernorm(x, axis=-1, eps=1e-05):
    m = x.mean(dim=axis, keepdim=True)
    v = x.var(dim=axis, keepdim=True, unbiased=False)
    return (x - m) / torch.sqrt(v + eps)

def _scatter_add(tgt, idx, src, axis=0):
    return tgt.clone().index_add_(axis, idx, src)

def _segment_sum(src, seg, n):
    out = torch.zeros((n,) + src.shape[1:], dtype=src.dtype)
    return out.index_add_(0, seg, src)

def _pad(x, pw):
    flat = tuple((v for pair in reversed(pw) for v in pair))
    return torch.nn.functional.pad(x, flat)
kernel_name = 'adaLN_cond__ssm_conv_s4__glu_gated_swiglu__prompt'

def setup_inputs():

    _w = lambda a: (lambda t: t.to(torch.bfloat16) if t.is_floating_point() else t)(torch.as_tensor(a))

    def _A_setup():
        B, L, D, N = (1, 2048, 512, 128)
        A_re = -(lambda _x: torch.exp(torch.as_tensor(_x)))((lambda *s: torch.randn(*s, dtype=torch.bfloat16) * 3.2)(D, N))
        A_im = torch.pi * (lambda *s: torch.randn(*s, dtype=torch.bfloat16) * 0.05)(D, N)
        B_re = (lambda *s: torch.randn(*s, dtype=torch.bfloat16) * 0.1)(D, N)
        B_im = (lambda *s: torch.randn(*s, dtype=torch.bfloat16) * 0.1)(D, N)
        C_re = (lambda *s: torch.randn(*s, dtype=torch.bfloat16) * 0.1)(D, N)
        C_im = (lambda *s: torch.randn(*s, dtype=torch.bfloat16) * 0.1)(D, N)
        log_dt = (lambda *s: torch.randn(*s, dtype=torch.bfloat16) * 0.05)(D) * 0.5 - 4.0
        Dskip = (lambda *s: torch.randn(*s, dtype=torch.bfloat16) * 0.05)(D)
        return {'u': _w((lambda *s: torch.randn(*s, dtype=torch.bfloat16) * 0.05)(B, L, D)), 'A_re': _w(A_re), 'A_im': _w(A_im), 'B_re': _w(B_re), 'B_im': _w(B_im), 'C_re': _w(C_re), 'C_im': _w(C_im), 'log_dt': _w(log_dt), 'Dskip': _w(Dskip)}
    _A_in = _A_setup()

    def _B_setup():
        B, S, D, F = (1, 2048, 512, 1792)
        return {'x': _w((lambda *s: torch.randn(*s, dtype=torch.bfloat16) * 0.05)(B, S, D)), 'w1': _w((lambda *s: torch.randn(*s, dtype=torch.bfloat16) * 0.4)(D, F) * (2.0 / D) ** 0.5), 'w3': _w((lambda *s: torch.randn(*s, dtype=torch.bfloat16) * 0.4)(D, F) * (2.0 / D) ** 0.5), 'w2': _w((lambda *s: torch.randn(*s, dtype=torch.bfloat16) * 0.4)(F, D) * (2.0 / F) ** 0.5)}
    _B_in = _B_setup()
    return {'A_u': _A_in['u'], 'A_A_re': _A_in['A_re'], 'A_A_im': _A_in['A_im'], 'A_B_re': _A_in['B_re'], 'A_B_im': _A_in['B_im'], 'A_C_re': _A_in['C_re'], 'A_C_im': _A_in['C_im'], 'A_log_dt': _A_in['log_dt'], 'A_Dskip': _A_in['Dskip'], 'B_x': _B_in['x'], 'B_w1': _B_in['w1'], 'B_w3': _B_in['w3'], 'B_w2': _B_in['w2'], 'c': _w((lambda *s: torch.randn(*s, dtype=torch.bfloat16) * 0.2)(1, 512) * 8.0), 'w_mod': _w((lambda *s: torch.randn(*s, dtype=torch.bfloat16) * 0.2)(512, 6 * 512) * 512 ** (-0.5) * 8.0)}

def reference(A_u, A_A_re, A_A_im, A_B_re, A_B_im, A_C_re, A_C_im, A_log_dt, A_Dskip, B_x, B_w1, B_w3, B_w2, c, w_mod):

    def _A_ref(u, A_re, A_im, B_re, B_im, C_re, C_im, log_dt, Dskip):
        Bsz, L, D = u.shape
        dt = torch.exp(log_dt)[:, None]
        A = A_re + 1j * A_im
        Bc = B_re + 1j * B_im
        Cc = C_re + 1j * C_im
        logdA = dt * A
        dB = dt * Bc
        l = (lambda x, dt: x.to(dt))(torch.arange(L), A_re.dtype)
        P = torch.exp(logdA[:, :, None] * l[None, None, :])
        K = torch.einsum('dn,dnl,dn->dl', Cc, P, dB).real
        L2 = 2 * L
        Kp = _pad(K, ((0, 0), (0, L)))
        up = (lambda x, *d: x.permute(*d))(u, 0, 2, 1)
        up = _pad(up, ((0, 0), (0, 0), (0, L)))
        Kf = (lambda x, axes=(-2, -1): torch.fft.rfft2(x.float(), dim=axes))(Kp[:, None, :], axes=(-2, -1))
        Uf = (lambda x, axes=(-2, -1): torch.fft.rfft2(x.float(), dim=axes))(up[:, :, None, :], axes=(-2, -1))
        Yf = Uf * Kf[None, :, :, :]
        y = (lambda x, s, axes=(-2, -1): torch.fft.irfft2(x, s=s, dim=axes).to(torch.bfloat16))(Yf, (1, L2), axes=(-2, -1))[:, :, 0, :L]
        y = (lambda x, *d: x.permute(*d))(y, 0, 2, 1) + u * Dskip[None, None, :]
        return y

    def _B_ref(x, w1, w3, w2):
        gate = torch.einsum('bsd,df->bsf', x, w1)
        up = torch.einsum('bsd,df->bsf', x, w3)
        h = _silu(gate) * up
        out = torch.einsum('bsf,fd->bsd', h, w2)
        return out
    x = A_u
    mod = (_silu(c) @ w_mod).reshape(c.shape[0], 6, 1, -1)
    sa, ca, ga, sb, cb, gb = (mod[:, 0].clone(), mod[:, 1].clone(), mod[:, 2].clone(), mod[:, 3].clone(), mod[:, 4].clone(), mod[:, 5].clone())
    h = _layernorm(x, axis=-1) * (1 + ca) + sa
    x = x + ga * _A_ref(h, A_A_re, A_A_im, A_B_re, A_B_im, A_C_re, A_C_im, A_log_dt, A_Dskip)
    h = _layernorm(x, axis=-1) * (1 + cb) + sb
    x = x + gb * _B_ref(h, B_w1, B_w3, B_w2)
    return x
```

```cpp
#include <hip/hip_runtime.h>
#include <hip/hip_bf16.h>

typedef unsigned short u16;
typedef unsigned u32;
typedef u32 v4u __attribute__((ext_vector_type(4)));
typedef u32 v2u __attribute__((ext_vector_type(2)));
typedef float v2f __attribute__((ext_vector_type(2)));
typedef float v4f __attribute__((ext_vector_type(4)));
typedef float v16f __attribute__((ext_vector_type(16)));
typedef __bf16 v8bf __attribute__((ext_vector_type(8)));
typedef __bf16 v2bf __attribute__((ext_vector_type(2)));
typedef short v4s __attribute__((ext_vector_type(4)));
#define GAS __attribute__((address_space(1)))
#define LAS __attribute__((address_space(3)))
#define DEV __device__ __forceinline__

constexpr int L_ = 2048, D_ = 512, N_ = 128, F_ = 1792;
constexpr size_t WS_XB = 0;
constexpr size_t WS_ST1 = WS_XB + (2u << 20);
constexpr size_t WS_MOD = WS_ST1 + 16384;
constexpr size_t WS_GB = WS_MOD + 49152;
constexpr size_t WS_FLAG = WS_GB + 2048;
constexpr size_t WS_K = WS_FLAG + 8192 + 6144;
constexpr int KROW = 2080;
constexpr size_t WS_W1P = WS_K + 512 * KROW * 2 + 4096;
constexpr size_t WS_W2P = WS_W1P + 16 * 33 * 7 * 1024;
constexpr size_t WS_X1 = WS_W2P + 8 * 112 * 2 * 1024;
constexpr size_t WS_ST2 = WS_X1 + (2u << 20);
constexpr size_t WS_H2 = WS_ST2 + 16 * 16 * 4 * 64 * 8;
constexpr size_t WS_END = WS_H2 + 224 * 2048 * 16;
#define TS(k, i) do {} while (0)

DEV float asf(u32 h) { return __builtin_bit_cast(float, h); }
DEV u32 asu(float f) { return __builtin_bit_cast(u32, f); }
DEV float bf2f(u32 h) { return __builtin_bit_cast(float, h << 16); }
DEV float bflo(u32 w) { return __builtin_bit_cast(float, w << 16); }
DEV float bfhi(u32 w) { return __builtin_bit_cast(float, w & 0xffff0000u); }
DEV u32 pk2(float lo, float hi) { v2f a = {lo, hi}; return __builtin_bit_cast(u32, __builtin_convertvector(a, v2bf)); }
DEV float rbf(float x) { return bflo(pk2(x, x)); }
DEV v4f mfma16(v4u a, v4u b, v4f c) { return __builtin_amdgcn_mfma_f32_16x16x32_bf16(__builtin_bit_cast(v8bf, a), __builtin_bit_cast(v8bf, b), c, 0, 0, 0); }
DEV v16f mfma32(v4u a, v4u b, v16f c) { return __builtin_amdgcn_mfma_f32_32x32x16_bf16(__builtin_bit_cast(v8bf, a), __builtin_bit_cast(v8bf, b), c, 0, 0, 0); }
DEV v2u trread(const char* p) { return __builtin_bit_cast(v2u, __builtin_amdgcn_ds_read_tr16_b64_v4i16((LAS v4s*)p)); }
DEV void dma16(const void* g, char* l) { __builtin_amdgcn_global_load_lds((const GAS void*)g, (LAS void*)l, 16, 0, 0); }
#define WAITV(n) asm volatile("s_waitcnt vmcnt(" #n ")" ::: "memory")
#define WAITL(n) asm volatile("s_waitcnt lgkmcnt(" #n ")" ::: "memory")
#define BAR() __builtin_amdgcn_s_barrier()
#define SCHED() __builtin_amdgcn_sched_barrier(0)
template <int CTRL> DEV float dppf(float x) { return __builtin_bit_cast(float, __builtin_amdgcn_update_dpp(0, __builtin_bit_cast(int, x), CTRL, 0xf, 0xf, true)); }
DEV float swap32sum(float x) { u32 u = __builtin_bit_cast(u32, x); auto r = __builtin_amdgcn_permlane32_swap(u, u, false, false); return asf(r[0]) + asf(r[1]); }
DEV float swap16sum(float x) { u32 u = __builtin_bit_cast(u32, x); auto r = __builtin_amdgcn_permlane16_swap(u, u, false, false); return asf(r[0]) + asf(r[1]); }
DEV float wavesum(float x) {
  x += dppf<0xB1>(x); x += dppf<0x4E>(x); x += dppf<0x141>(x); x += dppf<0x140>(x);
  x = swap16sum(x); return swap32sum(x);
}
DEV float modval(const char* ws, int idx) { v4f p = *(const v4f*)(ws + WS_MOD + (size_t)idx * 16); return rbf((p.x + p.y) + (p.z + p.w)); }

struct cf { float x, y; };
DEV cf cmul(cf a, cf b) { return cf{a.x * b.x - a.y * b.y, a.x * b.y + a.y * b.x}; }
DEV cf cexpm(float a, float b, float m) {
  float e = __builtin_amdgcn_exp2f(a * m), ph = __builtin_amdgcn_fractf(b * m);
  return cf{e * __builtin_amdgcn_cosf(ph), e * __builtin_amdgcn_sinf(ph)};
}

constexpr int TROW = 132;
__global__ void __launch_bounds__(512, 4)
k1_pre(const u16* __restrict__ x, const u16* __restrict__ Are, const u16* __restrict__ Aim, const u16* __restrict__ Bre,
       const u16* __restrict__ Bim, const u16* __restrict__ Cre, const u16* __restrict__ Cim, const u16* __restrict__ logdt,
       const u16* __restrict__ Dsk, const u16* __restrict__ cvec, const u16* __restrict__ wmod, char* __restrict__ ws) {
  __shared__ __attribute__((aligned(16))) char sm[2 * 64 * TROW * 4 + 2 * 648 * 2];
  const int tid = threadIdx.x, lane = tid & 63, w = __builtin_amdgcn_readfirstlane(tid >> 6);
  TS(0, 0);
  if (blockIdx.x < 256) {
    const int P = (blockIdx.x & 7) * 32 + (blockIdx.x >> 3);
    u32* tab = (u32*)sm;
    u16* kc = (u16*)(sm + 2 * 64 * TROW * 4);
    {
      const int ch = w >> 2, grp = (w >> 1) & 1, n = (w & 1) * 64 + lane, d = 2 * P + ch;
      const int o = d * N_ + n;
      float are = bf2f(Are[o]), aim = bf2f(Aim[o]);
      float dt = rbf(__builtin_amdgcn_exp2f(bf2f(logdt[d]) * 1.44269504089f));
      float a = (dt * are) * 1.44269504089f, b = (dt * aim) * 0.15915494309f;
      u32* t = tab + ch * 64 * TROW + n;
      if (grp == 0) {
        cf Bd = {dt * bf2f(Bre[o]), dt * bf2f(Bim[o])}, C = {bf2f(Cre[o]), bf2f(Cim[o])};
        cf z8 = cexpm(a, b, 8.f), z16 = cexpm(a, b, 16.f), z128 = cexpm(a, b, 128.f), Z = cexpm(a, b, 1024.f);
        cf G0 = cmul(C, Bd), G1 = cmul(G0, z8);
#pragma unroll
        for (int p = 0; p < 16; p += 2) {
          t[p * TROW] = pk2(G0.x, -G0.y); t[(p + 1) * TROW] = pk2(G1.x, -G1.y);
          t[(48 + p / 2) * TROW] = pk2(Z.x, Z.y);
          if (p < 14) { G0 = cmul(G0, z16); G1 = cmul(G1, z16); }
          Z = cmul(Z, z128);
        }
        t[56 * TROW] = pk2(Z.x, Z.y);
      } else {
        cf wv[8], Z[8];
        wv[1] = cexpm(a, b, 1.f); Z[1] = cexpm(a, b, 128.f); Z[4] = cexpm(a, b, 512.f);
        wv[2] = cmul(wv[1], wv[1]); wv[3] = cmul(wv[2], wv[1]); wv[4] = cmul(wv[2], wv[2]);
        wv[5] = cmul(wv[4], wv[1]); wv[6] = cmul(wv[4], wv[2]); wv[7] = cmul(wv[4], wv[3]);
        Z[2] = cmul(Z[1], Z[1]); Z[3] = cmul(Z[2], Z[1]); Z[5] = cmul(Z[4], Z[1]); Z[6] = cmul(Z[4], Z[2]); Z[7] = cmul(Z[4], Z[3]);
        u32* e = t + 16 * TROW;
        e[0] = pk2(1.f, 0.f);
#pragma unroll
        for (int i = 1; i < 8; ++i) e[i * TROW] = pk2(wv[i].x, wv[i].y);
        e[8 * TROW] = pk2(Z[1].x, Z[1].y);
#pragma unroll
        for (int i = 1; i < 8; ++i) { cf v = cmul(Z[1], wv[i]); e[(8 + i) * TROW] = pk2(v.x, v.y); }
#pragma unroll
        for (int q = 2; q < 4; ++q) {
          e[(16 + 4 * (q - 2)) * TROW] = pk2(Z[q].x, Z[q].y);
#pragma unroll
          for (int i = 1; i < 4; ++i) { cf v = cmul(Z[q], wv[2 * i]); e[(16 + 4 * (q - 2) + i) * TROW] = pk2(v.x, v.y); }
        }
#pragma unroll
        for (int q = 4; q < 8; ++q) {
          e[(24 + 2 * (q - 4)) * TROW] = pk2(Z[q].x, Z[q].y);
          cf v = cmul(Z[q], wv[4]); e[(25 + 2 * (q - 4)) * TROW] = pk2(v.x, v.y);
        }
      }
    }
    __syncthreads();
    TS(0, 1);
    if (w < 6) {
      const int ch = w >= 3, ct = w - 3 * ch, c = lane & 15, g = lane >> 4;
      const u32* ta = tab + ch * 64 * TROW + c * TROW + g * 4;
      const u32* tb = ta + (16 + 16 * ct) * TROW;
      v4f acc = {0.f, 0.f, 0.f, 0.f};
#pragma unroll
      for (int s = 0; s < 8; ++s) acc = mfma16(*(const v4u*)(ta + 16 * s), *(const v4u*)(tb + 16 * s), acc);
      const int col = 16 * ct + c;
      int base, step;
      if (col < 16) { base = (col >> 3) * 128 + (col & 7); step = 8; }
      else if (col < 24) { base = 256 + ((col - 16) >> 2) * 64 + (col & 3); step = 4; }
      else if (col < 32) { base = 384 + ((col - 24) >> 1) * 32 + (col & 1); step = 2; }
      else { base = 512 + (col - 32) * 16; step = 1; }
      if (col == 0 && g == 0) acc[0] += bf2f(Dsk[2 * P + ch]);
      u16* k = kc + ch * 648;
#pragma unroll
      for (int i = 0; i < 4; ++i) {
        int j = base + (4 * g + i) * step;
        if (j <= 640) k[j] = (u16)pk2(acc[i], 0.f);
      }
    } else if (tid >= 510) kc[(tid & 1) * 648 + 644] = 0;
    __syncthreads();
    TS(0, 2);
    {
      const int ch = tid >> 8, c = tid & 255;
      const u16* k = kc + ch * 648;
      u16* dst = (u16*)(ws + WS_K) + (size_t)(2 * P + ch) * KROW;
#pragma unroll
      for (int r = 0; r < 2; ++r) {
        if (r == 1 && c >= 4) break;
        const int q = r == 0 ? c + 4 : c;
        u32 o[4];
#pragma unroll
        for (int j = 0; j < 8; j += 2) {
          int l = 8 * q - 30 + j;
          u32 bb = pk2((float)l, (float)(l + 1));
          int i0 = (int)(bb & 0xffff) - (134 << 7) + 128, i1 = (int)(bb >> 16) - (134 << 7) + 128;
          if (l < 256) { i0 = l; i1 = l + 1; }
          if (l < 0) { i0 = 644; i1 = 644; }
          o[j / 2] = (u32)k[i0] | ((u32)k[i1] << 16);
        }
        *(v4u*)(dst + 8 * q) = v4u{o[0], o[1], o[2], o[3]};
      }
    }
  } else {
    const int j = blockIdx.x - 256;
    v4u xv = *(const v4u*)(x + (size_t)(8 * j + w) * D_ + lane * 8);
    const bool domod = j < 192;
    const int strip = j % 48, kq = j / 48, k0 = kq * 128 + w * 16 + (lane >> 3);
    v4u w0 = {}, w1 = {}; float c0 = 0.f, c1 = 0.f;
    if (domod) {
      const u16* wp = wmod + (size_t)k0 * 3072 + strip * 64 + (lane & 7) * 8;
      w0 = *(const v4u*)wp; w1 = *(const v4u*)(wp + 8 * 3072);
      c0 = bf2f(cvec[k0]); c1 = bf2f(cvec[k0 + 8]);
    }
    if (tid < 128) *(v4u*)(ws + WS_ST2 + (size_t)j * 2048 + tid * 16) = v4u{~0u, ~0u, ~0u, ~0u};
    if (j == 0) *(v4u*)(ws + WS_FLAG + tid * 16) = v4u{0u, 0u, 0u, 0u};
    *(v4u*)(sm + w * 1040 + lane * 16) = xv;
    float f[8] = {bflo(xv.x), bfhi(xv.x), bflo(xv.y), bfhi(xv.y), bflo(xv.z), bfhi(xv.z), bflo(xv.w), bfhi(xv.w)};
    float s = ((f[0] + f[1]) + (f[2] + f[3])) + ((f[4] + f[5]) + (f[6] + f[7]));
    float mu = wavesum(s) * (1.f / 512.f), q = 0.f;
#pragma unroll
    for (int i = 0; i < 8; ++i) { float dlt = f[i] - mu; q += dlt * dlt; }
    q = wavesum(q);
    if (lane == 0) *(v2f*)(ws + WS_ST1 + (size_t)(8 * j + w) * 8) = v2f{mu, __builtin_amdgcn_rsqf(q * (1.f / 512.f) + 1e-5f)};
    float* red = (float*)(sm + 8 * 1040);
    if (domod) {
      c0 = rbf(c0 * __builtin_amdgcn_rcpf(1.f + __builtin_amdgcn_exp2f(-1.44269504089f * c0)));
      c1 = rbf(c1 * __builtin_amdgcn_rcpf(1.f + __builtin_amdgcn_exp2f(-1.44269504089f * c1)));
      float a[8];
#pragma unroll
      for (int i = 0; i < 4; ++i) {
        a[2 * i] = c0 * bflo(w0[i]) + c1 * bflo(w1[i]); a[2 * i + 1] = c0 * bfhi(w0[i]) + c1 * bfhi(w1[i]);
      }
#pragma unroll
      for (int i = 0; i < 8; ++i) { a[i] += dppf<0x128>(a[i]); a[i] = swap16sum(a[i]); a[i] = swap32sum(a[i]); }
      if (lane < 8) { *(v4f*)(red + w * 64 + lane * 8) = v4f{a[0], a[1], a[2], a[3]}; *(v4f*)(red + w * 64 + lane * 8 + 4) = v4f{a[4], a[5], a[6], a[7]}; }
    }
    __syncthreads();
    {
      const int i = lane & 15;
      const char* p = sm + (i >> 2) * 1040 + (tid & ~15) * 2 + (i & 3) * 8;
      v2u a = trread(p), b = trread(p + 4 * 1040);
      *(v4u*)(ws + WS_XB + ((size_t)j * 512 + tid) * 16) = v4u{a.x, a.y, b.x, b.y};
    }
    if (domod && tid < 64) {
      float t = 0.f;
#pragma unroll
      for (int i = 0; i < 8; ++i) t += red[i * 64 + tid];
      *(float*)(ws + WS_MOD + (size_t)(strip * 64 + tid) * 16 + kq * 4) = t;
    }
  }
  TS(0, 3);
}

constexpr int KCOPY = 4160;
constexpr int UCOPY = (256 + 32) * 16 + 32;
constexpr int SM2_K = 0, SM2_U = 8 * KCOPY + 512, SM2_P = SM2_U + 4 * UCOPY, SM2_END = SM2_P + 32768;
__global__ void __launch_bounds__(512, 4)
k2_conv(const u16* __restrict__ w1, const u16* __restrict__ w3, const u16* __restrict__ w2, char* __restrict__ ws) {
  extern __shared__ __attribute__((aligned(16))) char sm[];
  const int tid = threadIdx.x, lane = tid & 63, w = __builtin_amdgcn_readfirstlane(tid >> 6);
  TS(1, 0);
  if (blockIdx.x < 256) {
    const int P = (blockIdx.x & 7) * 32 + (blockIdx.x >> 3);
    const int ch = tid & 1, tb = tid >> 1, d = 2 * P + ch;
    const char* xb = ws + WS_XB + ((size_t)tb * 512 + d) * 16;
    v4u xv = *(const v4u*)xb;
    const v4f* stp = (const v4f*)(ws + WS_ST1 + (size_t)tb * 64);
    v4f st[4] = {stp[0], stp[1], stp[2], stp[3]};
    float xp = 0.f; v2f sp = {0.f, 0.f};
    if (tb > 0) { xp = bf2f(*(const u16*)(xb - 8192 + 14)); sp = *(const v2f*)((const char*)stp - 8); }
    float sa = modval(ws, d), g1 = rbf(1.f + modval(ws, 512 + d)), ga = modval(ws, 1024 + d);
    {
      const char* kg = ws + WS_K + (size_t)(2 * P) * KROW * 2;
#pragma unroll
      for (int i = 0; i < 5; ++i) {
        int id = (i * 8 + w) * 64 + lane;
        if (i < 4 || id < 2080) {
          int cp = id / 260, c = id - cp * 260;
          dma16(kg + (cp >> 2) * (KROW * 2) + (cp & 3) * 4 + c * 16, sm + SM2_K + (i * 8 + w) * 1024);
        }
      }
    }
    float xf[8] = {bflo(xv.x), bfhi(xv.x), bflo(xv.y), bfhi(xv.y), bflo(xv.z), bfhi(xv.z), bflo(xv.w), bfhi(xv.w)};
    {
      float h[9];
      h[0] = tb > 0 ? (xp - sp.x) * sp.y * g1 + sa : 0.f;
#pragma unroll
      for (int i = 0; i < 8; ++i) h[i + 1] = (xf[i] - st[i >> 1][(i & 1) * 2]) * st[i >> 1][(i & 1) * 2 + 1] * g1 + sa;
      char* u0 = sm + SM2_U + (ch * 2) * UCOPY + (255 - tb) * 16;
      *(v4u*)u0 = v4u{pk2(h[7], h[6]), pk2(h[5], h[4]), pk2(h[3], h[2]), pk2(h[1], h[0])};
      *(v4u*)(u0 + UCOPY) = v4u{pk2(h[8], h[7]), pk2(h[6], h[5]), pk2(h[4], h[3]), pk2(h[2], h[1])};
      if (tid < 128) *(v4u*)(sm + SM2_U + (tid >> 5) * UCOPY + (256 + (tid & 31)) * 16) = v4u{0u, 0u, 0u, 0u};
    }
    WAITV(0);
    __syncthreads();
    TS(1, 1);
    {
      const int wc = w & 1, hh = (w >> 1) & 1, as = w >> 2;
      const int m = lane & 15, g = lane >> 4;
      const char* pa = sm + SM2_K + (wc * 4 + (m & 3)) * KCOPY + (g + (m >> 2) + hh * 16) * 16;
      const char* pb = sm + SM2_U + (wc * 2 + (m & 1)) * UCOPY + (252 - 4 * (m >> 1) + g + hh * 16) * 16;
      v4f acc[4];
#pragma unroll
      for (int i = 0; i < 4; ++i) acc[i] = v4f{0.f, 0.f, 0.f, 0.f};
      if (as == 0) {
#pragma unroll 2
        for (int r = 0; r < 4; ++r) {
          v4u A[8], B[8];
#pragma unroll
          for (int i = 0; i < 8; ++i) { A[i] = *(const v4u*)(pa + r * 64 + i * 512); B[i] = *(const v4u*)(pb + r * 64 - i * 512); }
          acc[0] = mfma16(A[0], B[0], acc[0]);
#pragma unroll
          for (int q = 0; q <= 3; ++q) acc[1] = mfma16(A[q], B[3 - q], acc[1]);
#pragma unroll
          for (int q = 0; q <= 4; ++q) acc[2] = mfma16(A[q], B[4 - q], acc[2]);
#pragma unroll
          for (int q = 0; q <= 7; ++q) acc[3] = mfma16(A[q], B[7 - q], acc[3]);
        }
      } else {
#pragma unroll 2
        for (int r = 0; r < 4; ++r) {
          v4u A[7], B[7];
#pragma unroll
          for (int i = 0; i < 7; ++i) { A[i] = *(const v4u*)(pa + r * 64 + i * 512); B[i] = *(const v4u*)(pb + r * 64 - i * 512); }
#pragma unroll
          for (int q = 0; q <= 1; ++q) acc[0] = mfma16(A[q], B[1 - q], acc[0]);
#pragma unroll
          for (int q = 0; q <= 2; ++q) acc[1] = mfma16(A[q], B[2 - q], acc[1]);
#pragma unroll
          for (int q = 0; q <= 5; ++q) acc[2] = mfma16(A[q], B[5 - q], acc[2]);
#pragma unroll
          for (int q = 0; q <= 6; ++q) acc[3] = mfma16(A[q], B[6 - q], acc[3]);
        }
      }
      char* pp = sm + SM2_P + (hh * 2 + (m & 1)) * 8192 + ((4 * (m >> 1) + g) * 2 + wc) * 16;
#pragma unroll
      for (int i = 0; i < 4; ++i) {
        const int al = as == 0 ? (i == 0 ? 0 : i == 1 ? 3 : i == 2 ? 4 : 7) : (i == 0 ? 1 : i == 1 ? 2 : i == 2 ? 5 : 6);
        *(v4f*)(pp + al * 1024) = acc[i];
      }
    }
    __syncthreads();
    TS(1, 2);
    {
      const char* pp = sm + SM2_P + tid * 16;
      v4f e0 = *(const v4f*)pp + *(const v4f*)(pp + 16384), e1 = *(const v4f*)(pp + 8192) + *(const v4f*)(pp + 24576);
      float r[8];
#pragma unroll
      for (int i = 0; i < 4; ++i) { r[2 * i] = xf[2 * i] + ga * e0[i]; r[2 * i + 1] = xf[2 * i + 1] + ga * e1[i]; }
      char* dst = ws + WS_X1 + ((size_t)(P >> 2) * 2048 + tb * 8 + ch * 4) * 16 + (P & 3) * 4;
#pragma unroll
      for (int i = 0; i < 4; ++i) {
        float give = ch ? r[i] : r[4 + i], keep = ch ? r[4 + i] : r[i];
        float got = dppf<0xB1>(give);
        *(u32*)(dst + i * 16) = ch ? pk2(got, keep) : pk2(keep, got);
      }
    }
  } else if (blockIdx.x < 256 + 112) {
    const int jb = blockIdx.x - 256;
    const int j = (jb & 7) * 14 + (jb >> 3), fs = j / 7, tj = j - fs * 7, f0 = j * 16;
#pragma unroll
    for (int i = 0; i < 4; ++i) {
      int id = i * 512 + tid, k = (id >> 1) & 511;
      dma16((i < 2 ? w1 : w3) + (size_t)k * F_ + f0 + (id & 1) * 8, sm + (i * 8 + w) * 1024);
    }
    float* gs = (float*)(sm + 32768);
    gs[tid] = rbf(1.f + modval(ws, 4 * 512 + tid));
    gs[512 + tid] = modval(ws, 3 * 512 + tid);
    if (jb == 0) *(float*)(ws + WS_GB + tid * 4) = modval(ws, 5 * 512 + tid);
    WAITV(0);
    __syncthreads();
    const int mat = w >> 2, kb = (w & 3) * 128, m = lane & 15, g = lane >> 4;
    const int pq = m >> 2, pp = m & 3, pc = ((pp & 1) << 1) | (pp >> 1);
    const char* src = sm + mat * 16384 + (kb + 8 * g + pq) * 32 + pc * 8;
    char* dst = ws + WS_W1P + (((size_t)fs * 33 + kb / 16 + (g >> 1)) * 7 + tj) * 1024 + ((g & 1) * 32 + mat * 16 + m) * 16;
    float S = 0.f, T = 0.f;
#pragma unroll
    for (int b = 0; b < 4; ++b) {
      v2u a0 = trread(src + b * 1024), a1 = trread(src + b * 1024 + 128);
      const float* gp = gs + kb + 32 * b + 8 * g;
      v4f g0 = *(const v4f*)gp, g1 = *(const v4f*)(gp + 4), s0 = *(const v4f*)(gp + 512), s1 = *(const v4f*)(gp + 516);
      float v[8] = {bflo(a0.x), bfhi(a0.x), bflo(a0.y), bfhi(a0.y), bflo(a1.x), bfhi(a1.x), bflo(a1.y), bfhi(a1.y)};
      v4u o = {pk2(v[0] * g0[0], v[1] * g0[1]), pk2(v[2] * g0[2], v[3] * g0[3]), pk2(v[4] * g1[0], v[5] * g1[1]), pk2(v[6] * g1[2], v[7] * g1[3])};
      *(v4u*)(dst + (size_t)b * 2 * 7 * 1024) = o;
#pragma unroll
      for (int i = 0; i < 4; ++i) { S += bflo(o[i]) + bfhi(o[i]); T += v[i] * s0[i] + v[4 + i] * s1[i]; }
    }
    S = swap32sum(swap16sum(S)); T = swap32sum(swap16sum(T));
    float* red = gs + 1024;
    if (lane < 16) { red[w * 32 + lane] = S; red[w * 32 + 16 + lane] = T; }
    __syncthreads();
    if (tid < 64) {
      v4u o = {0u, 0u, 0u, 0u};
      if (tid < 32) {
        const float* r = red + (tid >> 4) * 128 + (tid & 15);
        float s = (r[0] + r[32]) + (r[64] + r[96]), t = (r[16] + r[48]) + (r[80] + r[112]);
        float sh = rbf(s), th = rbf(t);
        o = v4u{pk2(sh, sh), pk2(s - sh, th), pk2(th, t - th), 0u};
      }
      *(v4u*)(ws + WS_W1P + (((size_t)fs * 33 + 32) * 7 + tj) * 1024 + tid * 16) = o;
    }
  } else {
    const int jb = blockIdx.x - 368, ct = jb & 7, kr = jb >> 3;
#pragma unroll
    for (int i = 0; i < 4; ++i) {
      int id = i * 512 + tid;
      if (i < 3 || w < 4) dma16(w2 + (size_t)(kr * 224 + (id >> 3)) * D_ + ct * 64 + (id & 7) * 8, sm + (i * 8 + w) * 1024);
    }
    WAITV(0);
    __syncthreads();
    const int i16 = lane & 15, q = i16 >> 2, pc = ((lane >> 4) & 1) * 4 + (i16 & 3), h = lane >> 5;
    const int dp = (pc & 1) * 4 + (pc >> 1);
    const char* src = sm + (8 * h + q) * 128 + dp * 8;
#pragma unroll
    for (int i = 0; i < 4; ++i) {
      const int fr = i * 8 + w;
      if (fr < 28) {
        const char* s = src + (fr >> 1) * 2048 + (fr & 1) * 64;
        v2u a0 = trread(s), a1 = trread(s + 512);
        *(v4u*)(ws + WS_W2P + (((size_t)ct * 112 + kr * 14) * 2 + fr) * 1024 + lane * 16) = v4u{a0.x, a0.y, a1.x, a1.y};
      }
    }
  }
  TS(1, 3);
}

constexpr int CHB = 28 * 1024;
constexpr int NSLOT = 4;
constexpr int SM3_ST = NSLOT * CHB, SM3_END = SM3_ST + 7 * 1024;
__global__ void __launch_bounds__(256, 1)
k3_mlp(char* __restrict__ ws, u16* __restrict__ out) {
  extern __shared__ __attribute__((aligned(16))) char sm[];
  const int tid = threadIdx.x, lane = tid & 63, w = __builtin_amdgcn_readfirstlane(tid >> 6);
  const int b = blockIdx.x, xcd = b & 7, bi = b >> 3;
  const int rb = (xcd & 1) * 8 + (bi & 7), fs = (xcd >> 1) * 4 + (bi >> 3);
  const int q = (fs + 2 * rb) & 15, rot = q >> 1;
  const int tl = lane & 31, h = lane >> 5;
  TS(2, 0);
  __amdgpu_buffer_rsrc_t rws = __builtin_amdgcn_make_buffer_rsrc((void*)ws, 0, (int)WS_END, 0x00020000);
  const int tok = rb * 128 + w * 32 + tl;
  const int xoff = (int)WS_X1 + (h * 2048 + tok) * 16;
  const char* wsrc = ws + WS_W1P + (size_t)fs * 33 * 7 * 1024 + w * 1024 + lane * 16;
  v4u X[32];
  v16f acc[7];
#pragma unroll
  for (int i = 0; i < 7; ++i)
#pragma unroll
    for (int j = 0; j < 16; ++j) acc[i][j] = 0.f;
#define LOADX(c) do { const int cc_ = ((c) + rot) & 7; _Pragma("unroll") for (int s_ = 0; s_ < 4; ++s_) \
    X[(c) * 4 + s_] = __builtin_amdgcn_raw_buffer_load_b128(rws, xoff + s_ * 65536, cc_ * 262144, 0); } while (0)
#define LOADW(c) do { const int cc_ = ((c) + rot) & 7; _Pragma("unroll") for (int i_ = 0; i_ < 7; ++i_) \
    dma16(wsrc + cc_ * CHB + i_ * 4096, sm + ((c) % NSLOT) * CHB + (i_ * 4 + w) * 1024); } while (0)
  LOADX(0); LOADW(0); LOADX(1); LOADW(1);
  const int t2 = (xcd >> 1) * 8 + (bi & 7), ct = (xcd & 1) * 4 + (bi >> 3);
  const int w2off = (int)WS_W2P + ((ct * 112 + w * 28) * 2) * 1024 + lane * 16;
  v4u W2[56];
  v2u sg[16];
  const int sgoff = (int)WS_ST2 + ((rb * 16) * 4 + w) * 512 + lane * 8;
  WAITV(11); BAR();
#pragma unroll
  for (int c = 0; c < 8; ++c) {
    const char* wl = sm + (c % NSLOT) * CHB + lane * 16;
    if (c + 2 < 8) { LOADX(c + 2); LOADW(c + 2); }
    if (c == 6) {
      if (w < 3) { dma16(wsrc + 8 * CHB, sm + SM3_ST + w * 1024); dma16(wsrc + 8 * CHB + 4096, sm + SM3_ST + (4 + w) * 1024); }
      else { dma16(wsrc + 8 * CHB, sm + SM3_ST + w * 1024); dma16(wsrc + 8 * CHB, sm + SM3_ST + w * 1024); }
#pragma unroll
      for (int i = 0; i < 16; ++i) sg[i] = __builtin_amdgcn_raw_buffer_load_b64(rws, sgoff + i * 2048, 0, 16);
    }
    if (c == 7) {
#pragma unroll
      for (int i = 0; i < 24; ++i) W2[i] = __builtin_amdgcn_raw_buffer_load_b128(rws, w2off + i * 1024, 0, 0);
    }
#pragma unroll
    for (int s = 0; s < 4; ++s) {
#pragma unroll
      for (int t = 0; t < 7; ++t) acc[t] = mfma32(*(const v4u*)(wl + (s * 7 + t) * 1024), X[c * 4 + s], acc[t]);
    }
    if (c == 0) {
      v4u a = (q & 1) ? X[2] : X[0], bq = (q & 1) ? X[3] : X[1];
      float v[16];
#pragma unroll
      for (int i = 0; i < 4; ++i) { v[2 * i] = bflo(a[i]); v[2 * i + 1] = bfhi(a[i]); v[8 + 2 * i] = bflo(bq[i]); v[9 + 2 * i] = bfhi(bq[i]); }
      float s = 0.f, m2 = 0.f;
#pragma unroll
      for (int i = 0; i < 16; ++i) s += v[i];
      s *= 0.0625f;
#pragma unroll
      for (int i = 0; i < 16; ++i) { float dl = v[i] - s; m2 += dl * dl; }
      v2u gq = {__builtin_bit_cast(u32, s), __builtin_bit_cast(u32, m2) & 0x7fffffffu};
      __builtin_amdgcn_raw_buffer_store_b64(gq, rws, sgoff + q * 2048, 0, 16);
    }
    if (c == 0) { WAITV(12); } else if (c < 6) { WAITV(11); } else if (c == 6) { WAITV(18); } else { WAITV(40); }
    BAR();
  }
  TS(2, 1);
  for (int spin = 0; spin < 1000000; ++spin) {
    u32 bad = 0;
#pragma unroll
    for (int i = 0; i < 16; ++i) bad |= sg[i].y;
    if (!__any(bad >> 31)) break;
    asm volatile("" ::: "memory");
#pragma unroll
    for (int i = 0; i < 16; ++i) sg[i] = __builtin_amdgcn_raw_buffer_load_b64(rws, sgoff + i * 2048, 0, 16);
  }
  float mu = 0.f, m2 = 0.f;
#pragma unroll
  for (int i = 0; i < 16; ++i) mu += asf(sg[i].x);
  mu = swap32sum(mu) * (1.f / 32.f);
#pragma unroll
  for (int i = 0; i < 16; ++i) { float dl = asf(sg[i].x) - mu; m2 += asf(sg[i].y) + 16.f * dl * dl; }
  const float var = swap32sum(m2) * (1.f / 512.f) + 1e-5f;
  const float rs = __builtin_amdgcn_rsqf(var), sd = var * rs;
  {
    float mh = rbf(mu), sh = rbf(sd);
    v4u bs = {pk2(-mh, mh - mu), pk2(-mh, sh), pk2(sd - sh, sh), 0u};
    if (h) bs = v4u{0u, 0u, 0u, 0u};
#pragma unroll
    for (int t = 0; t < 7; ++t) acc[t] = mfma32(*(const v4u*)(sm + SM3_ST + t * 1024 + lane * 16), bs, acc[t]);
  }
  {
    const float ne = -1.44269504089f * rs;
    const int hoff = (int)WS_H2 + ((fs * 14 + h) * 2048 + tok) * 16;
#pragma unroll
    for (int t = 0; t < 7; ++t) {
      float o[8];
#pragma unroll
      for (int i = 0; i < 8; ++i) {
        float g = acc[t][i], u = acc[t][8 + i];
        float e = __builtin_amdgcn_exp2f(g * ne);
        o[i] = (g * u) * __builtin_amdgcn_rcpf(e * var + var);
      }
      __builtin_amdgcn_raw_buffer_store_b128(v4u{pk2(o[0], o[1]), pk2(o[2], o[3]), pk2(o[4], o[5]), pk2(o[6], o[7])}, rws, hoff + t * 65536, 0, 16);
    }
  }
  WAITV(0);
  BAR();
  if (tid == 0) __hip_atomic_store((GAS u32*)(ws + WS_FLAG + (rb * 4 + (fs >> 2)) * 128 + (fs & 3) * 4), 1u, __ATOMIC_RELAXED, __HIP_MEMORY_SCOPE_AGENT);
  TS(2, 2);
#pragma unroll
  for (int i = 24; i < 56; ++i) W2[i] = __builtin_amdgcn_raw_buffer_load_b128(rws, w2off + i * 1024, 0, 0);
  const int dt = w >> 1, tt = w & 1, tok2 = t2 * 64 + tt * 32 + tl, d0 = ct * 64 + dt * 32 + h * 16;
  v4u xr0 = __builtin_amdgcn_raw_buffer_load_b128(rws, (int)WS_X1 + ((d0 >> 3) * 2048 + tok2) * 16, 0, 0);
  v4u xr1 = __builtin_amdgcn_raw_buffer_load_b128(rws, (int)WS_X1 + (((d0 >> 3) + 1) * 2048 + tok2) * 16, 0, 0);
  v4u gb[4];
#pragma unroll
  for (int i = 0; i < 4; ++i) gb[i] = __builtin_amdgcn_raw_buffer_load_b128(rws, (int)WS_GB + d0 * 4 + i * 16, 0, 0);
  {
    const int foff = (int)WS_FLAG + ((t2 >> 1) * 4 + w) * 128;
    for (int spin = 0; spin < 1000000; ++spin) {
      asm volatile("" ::: "memory");
      v4u f = __builtin_amdgcn_raw_buffer_load_b128(rws, foff, 0, 16);
      if (__builtin_amdgcn_readfirstlane(f.x & f.y & f.z & f.w) == 1u) break;
      __builtin_amdgcn_s_sleep(1);
    }
    asm volatile("" ::: "memory");
  }
  TS(2, 3);
  v16f a2[4];
#pragma unroll
  for (int i = 0; i < 4; ++i)
#pragma unroll
    for (int j = 0; j < 16; ++j) a2[i][j] = 0.f;
  {
    const int hoff = (int)WS_H2 + ((w * 56 + h) * 2048 + t2 * 64 + tl) * 16;
    v4u H[56];
#pragma unroll
    for (int i = 0; i < 40; ++i) H[i] = __builtin_amdgcn_raw_buffer_load_b128(rws, hoff + (i & 1) * 512, (i >> 1) * 65536, 16);
#pragma unroll
    for (int s = 0; s < 28; ++s) {
      if (2 * s + 40 < 56) {
        H[2 * s + 40] = __builtin_amdgcn_raw_buffer_load_b128(rws, hoff, (s + 20) * 65536, 16);
        H[2 * s + 41] = __builtin_amdgcn_raw_buffer_load_b128(rws, hoff + 512, (s + 20) * 65536, 16);
      }
      a2[0] = mfma32(W2[2 * s], H[2 * s], a2[0]);
      a2[1] = mfma32(W2[2 * s], H[2 * s + 1], a2[1]);
      a2[2] = mfma32(W2[2 * s + 1], H[2 * s], a2[2]);
      a2[3] = mfma32(W2[2 * s + 1], H[2 * s + 1], a2[3]);
    }
  }
  TS(2, 4);
  v16f mine;
#pragma unroll
  for (int u = 0; u < 4; ++u) {
    if (u == w) mine = a2[u];
    else {
#pragma unroll
      for (int i = 0; i < 4; ++i)
        *(v4f*)(sm + ((u * 4 + w) * 4 + i) * 1024 + lane * 16) = v4f{a2[u][4 * i], a2[u][4 * i + 1], a2[u][4 * i + 2], a2[u][4 * i + 3]};
    }
  }
  __syncthreads();
#pragma unroll
  for (int sw = 1; sw < 4; ++sw) {
    const int src = (w + sw) & 3;
#pragma unroll
    for (int i = 0; i < 4; ++i) {
      v4f v = *(const v4f*)(sm + ((w * 4 + src) * 4 + i) * 1024 + lane * 16);
#pragma unroll
      for (int j = 0; j < 4; ++j) mine[4 * i + j] += v[j];
    }
  }
  {
    u32 o[8];
#pragma unroll
    for (int i = 0; i < 8; ++i) {
      u32 xw = i < 4 ? xr0[i] : xr1[i - 4];
      float g0 = asf(gb[i >> 1][(i & 1) * 2]), g1 = asf(gb[i >> 1][(i & 1) * 2 + 1]);
      o[i] = pk2(bflo(xw) + g0 * mine[2 * i], bfhi(xw) + g1 * mine[2 * i + 1]);
    }
    u16* op = out + (size_t)tok2 * D_ + d0;
    *(v4u*)op = v4u{o[0], o[1], o[2], o[3]};
    *(v4u*)(op + 8) = v4u{o[4], o[5], o[6], o[7]};
  }
  TS(2, 5);
}

extern "C" void kernel_launch(void* const* d_in, const int* in_sizes, int n_in, void* d_out, int out_size, void* d_ws,
                              size_t ws_size, hipStream_t stream) {
  const u16* const* in = (const u16* const*)d_in;
  char* ws = (char*)d_ws;
  (void)hipFuncSetAttribute((const void*)k2_conv, hipFuncAttributeMaxDynamicSharedMemorySize, SM2_END);
  (void)hipFuncSetAttribute((const void*)k3_mlp, hipFuncAttributeMaxDynamicSharedMemorySize, SM3_END);
  k1_pre<<<512, 512, 0, stream>>>(in[0], in[1], in[2], in[3], in[4], in[5], in[6], in[7], in[8], in[13], in[14], ws);
  k2_conv<<<256 + 112 + 64, 512, SM2_END, stream>>>(in[10], in[11], in[12], ws);
  k3_mlp<<<256, 256, SM3_END, stream>>>(ws, (u16*)d_out);
}
```

```cpp
#include <hip/hip_runtime.h>
#include <hip/hip_bf16.h>

typedef unsigned short u16;
typedef unsigned u32;
typedef u32 v4u __attribute__((ext_vector_type(4)));
typedef u32 v2u __attribute__((ext_vector_type(2)));
typedef u32 v8u __attribute__((ext_vector_type(8)));
typedef u32 v16u __attribute__((ext_vector_type(16)));
typedef float v2f __attribute__((ext_vector_type(2)));
typedef float v4f __attribute__((ext_vector_type(4)));
typedef float v16f __attribute__((ext_vector_type(16)));
typedef __bf16 v8bf __attribute__((ext_vector_type(8)));
typedef __bf16 v2bf __attribute__((ext_vector_type(2)));
typedef short v4s __attribute__((ext_vector_type(4)));
#define GAS __attribute__((address_space(1)))
#define LAS __attribute__((address_space(3)))
#define CAS __attribute__((address_space(4)))
#define DEV __device__ __forceinline__

constexpr int L_ = 2048, D_ = 512, N_ = 128, F_ = 1792;
constexpr size_t WS_EP = 0;
constexpr size_t WS_ST1 = 4096;
constexpr size_t WS_MOD = WS_ST1 + 16384;
constexpr size_t WS_GB = WS_MOD + 49152;
constexpr size_t WS_FLAG = WS_GB + 2048;
constexpr int W1FS = 16 * 14 * 1024 + 8192;
constexpr size_t WS_XB = 81920;
constexpr size_t WS_W1P = WS_XB + (4u << 20);
constexpr size_t WS_W2P = WS_W1P + 16 * W1FS;
constexpr size_t WS_X1 = WS_W2P + 8 * 56 * 4 * 1024;
constexpr size_t WS_ST2 = WS_X1 + (2u << 20);
constexpr size_t WS_H2 = WS_ST2 + 16 * 16 * 4 * 16 * 16;
constexpr size_t WS_END = WS_H2 + 224 * 2048 * 16;

DEV float asf(u32 h) { return __builtin_bit_cast(float, h); }
DEV u32 asu(float f) { return __builtin_bit_cast(u32, f); }
DEV float bf2f(u32 h) { return __builtin_bit_cast(float, h << 16); }
DEV float bflo(u32 w) { return __builtin_bit_cast(float, w << 16); }
DEV float bfhi(u32 w) { return __builtin_bit_cast(float, w & 0xffff0000u); }
DEV u32 pk2(float lo, float hi) { v2f a = {lo, hi}; return __builtin_bit_cast(u32, __builtin_convertvector(a, v2bf)); }
DEV float rbf(float x) { return bflo(pk2(x, x)); }
DEV v4f mfma16(v4u a, v4u b, v4f c) { return __builtin_amdgcn_mfma_f32_16x16x32_bf16(__builtin_bit_cast(v8bf, a), __builtin_bit_cast(v8bf, b), c, 0, 0, 0); }
DEV v16f mfma32(v4u a, v4u b, v16f c) { return __builtin_amdgcn_mfma_f32_32x32x16_bf16(__builtin_bit_cast(v8bf, a), __builtin_bit_cast(v8bf, b), c, 0, 0, 0); }
DEV v2u trread(const char* p) { return __builtin_bit_cast(v2u, __builtin_amdgcn_ds_read_tr16_b64_v4i16((LAS v4s*)p)); }
DEV void dma16(const void* g, char* l) { __builtin_amdgcn_global_load_lds((const GAS void*)g, (LAS void*)l, 16, 0, 0); }
#define WAITV(n) asm volatile("s_waitcnt vmcnt(" #n ")" ::: "memory")
#define WAITL(n) asm volatile("s_waitcnt lgkmcnt(" #n ")" ::: "memory")
#define BAR() asm volatile("s_waitcnt lgkmcnt(0)\n\ts_barrier" ::: "memory")
#define SCHED() __builtin_amdgcn_sched_barrier(0)
template <int CTRL> DEV float dppf(float x) { return __builtin_bit_cast(float, __builtin_amdgcn_update_dpp(0, __builtin_bit_cast(int, x), CTRL, 0xf, 0xf, true)); }
DEV float swap32sum(float x) { u32 u = __builtin_bit_cast(u32, x); auto r = __builtin_amdgcn_permlane32_swap(u, u, false, false); return asf(r[0]) + asf(r[1]); }
DEV float swap16sum(float x) { u32 u = __builtin_bit_cast(u32, x); auto r = __builtin_amdgcn_permlane16_swap(u, u, false, false); return asf(r[0]) + asf(r[1]); }
DEV float halfsum(float x) {
  x += dppf<0xB1>(x); x += dppf<0x4E>(x); x += dppf<0x141>(x); x += dppf<0x140>(x);
  return swap16sum(x);
}
DEV float silu_bf(u32 h) { float c = bf2f(h); return rbf(c * __builtin_amdgcn_rcpf(1.f + __builtin_amdgcn_exp2f(-1.44269504089f * c))); }

DEV v2f cmul(v2f a, v2f b) {
  v2f r; const v2f t = a.xx * b;
  asm("v_pk_fma_f32 %0, %1, %2, %3 op_sel:[1,1,0] op_sel_hi:[1,0,1] neg_lo:[1,0,0]" : "=v"(r) : "v"(a), "v"(b), "v"(t));
  return r;
}
DEV v2f cexpm(float a, float b, float m) {
  float e = __builtin_amdgcn_exp2f(a * m), ph = __builtin_amdgcn_fractf(b * m);
  return v2f{__builtin_amdgcn_cosf(ph), __builtin_amdgcn_sinf(ph)} * e;
}

DEV void adma(const void* base, int voff, int lds) {
  asm volatile("s_mov_b32 m0, %2\n\ts_nop 0\n\tglobal_load_lds_dwordx4 %1, %0" :: "s"(base), "v"(voff), "s"(lds) : "memory");
}

#define K3_ASM \
"buffer_load_dwordx4 v[128:131], %[lb0], %[srd], %[sXs] offen\n\t" \
"buffer_load_dwordx4 v[132:135], %[lb0], %[srd], %[sXs] offen offset:1024\n\t" \
"s_add_u32 s60, %[sX], 0\n\t" \
"buffer_load_dwordx4 a[0:3], %[lb0], %[srd], s60 offen offset:0\n\t" \
"buffer_load_dwordx4 a[4:7], %[lb0], %[srd], s60 offen offset:1024\n\t" \
"s_add_u32 s61, %[sW1], 0\n\t" \
"s_add_u32 m0, %[w1], 0\n\t" \
"s_nop 0\n\t" \
"buffer_load_dwordx4 %[lb0], %[srd], s61 offen lds\n\t" \
"s_add_u32 s61, %[sW1], 4096\n\t" \
"s_add_u32 m0, %[w1], 4096\n\t" \
"s_nop 0\n\t" \
"buffer_load_dwordx4 %[lb0], %[srd], s61 offen lds\n\t" \
"s_add_u32 s61, %[sW1], 8192\n\t" \
"s_add_u32 m0, %[w1], 8192\n\t" \
"s_nop 0\n\t" \
"buffer_load_dwordx4 %[lb0], %[srd], s61 offen lds\n\t" \
"s_add_u32 s61, %[sW1], 12288\n\t" \
"s_add_u32 m0, %[w1], 12288\n\t" \
"s_nop 0\n\t" \
"buffer_load_dwordx4 %[lb0], %[srd], s61 offen lds\n\t" \
"s_add_u32 s60, %[sX], 131072\n\t" \
"buffer_load_dwordx4 a[8:11], %[lb0], %[srd], s60 offen offset:0\n\t" \
"buffer_load_dwordx4 a[12:15], %[lb0], %[srd], s60 offen offset:1024\n\t" \
"s_add_u32 s60, %[sX], 262144\n\t" \
"buffer_load_dwordx4 a[16:19], %[lb0], %[srd], s60 offen offset:0\n\t" \
"buffer_load_dwordx4 a[20:23], %[lb0], %[srd], s60 offen offset:1024\n\t" \
"s_add_u32 s61, %[sW1], 16384\n\t" \
"s_add_u32 m0, %[w1], 16384\n\t" \
"s_nop 0\n\t" \
"buffer_load_dwordx4 %[lb0], %[srd], s61 offen lds\n\t" \
"s_add_u32 s61, %[sW1], 20480\n\t" \
"s_add_u32 m0, %[w1], 20480\n\t" \
"s_nop 0\n\t" \
"buffer_load_dwordx4 %[lb0], %[srd], s61 offen lds\n\t" \
"s_add_u32 s61, %[sW1], 24576\n\t" \
"s_add_u32 m0, %[w1], 24576\n\t" \
"s_nop 0\n\t" \
"buffer_load_dwordx4 %[lb0], %[srd], s61 offen lds\n\t" \
"s_add_u32 s60, %[sX], 393216\n\t" \
"buffer_load_dwordx4 a[24:27], %[lb0], %[srd], s60 offen offset:0\n\t" \
"buffer_load_dwordx4 a[28:31], %[lb0], %[srd], s60 offen offset:1024\n\t" \
"s_add_u32 s60, %[sX], 524288\n\t" \
"buffer_load_dwordx4 a[32:35], %[lb0], %[srd], s60 offen offset:0\n\t" \
"buffer_load_dwordx4 a[36:39], %[lb0], %[srd], s60 offen offset:1024\n\t" \
"s_add_u32 s61, %[sW7], 28672\n\t" \
"s_add_u32 m0, %[w7], 28672\n\t" \
"s_nop 0\n\t" \
"buffer_load_dwordx4 %[lb0], %[srd], s61 offen offset:0 lds\n\t" \
"buffer_load_dwordx4 %[lb0], %[srd], s61 offen offset:1024 lds\n\t" \
"s_add_u32 s60, %[sX], 655360\n\t" \
"buffer_load_dwordx4 a[40:43], %[lb0], %[srd], s60 offen offset:0\n\t" \
"s_mov_b32 s63, 0\n\t" \
"v_and_b32 v19, 0x1f0, %[lb0]\n\t" \
"v_add_u32 v19, 143360, v19\n\t" \
"s_add_u32 s68, %[sHl], 1048576\n\t" \
"s_add_u32 s69, %[sHl], 1835008\n\t" \
"v_mov_b32 v18, 1\n\t" \
"s_cmp_eq_u32 %[w1], 0\n\t" \
"s_cselect_b64 s[70:71], 1, 0\n\t" \
"s_lshl_b32 s67, %[w1], 3\n\t" \
"s_add_u32 s67, s67, 86016\n\t" \
"s_waitcnt vmcnt(20)\n\t" \
"v_lshlrev_b32 v96, 16, v128\n\t" \
"v_and_b32 v97, 0xffff0000, v128\n\t" \
"v_lshlrev_b32 v98, 16, v130\n\t" \
"v_and_b32 v99, 0xffff0000, v130\n\t" \
"v_lshlrev_b32 v100, 16, v132\n\t" \
"v_and_b32 v101, 0xffff0000, v132\n\t" \
"v_lshlrev_b32 v102, 16, v134\n\t" \
"v_and_b32 v103, 0xffff0000, v134\n\t" \
"buffer_load_dwordx4 a[44:47], %[lb0], %[srd], s60 offen offset:1024\n\t" \
"v_lshlrev_b32 v104, 16, v129\n\t" \
"v_and_b32 v105, 0xffff0000, v129\n\t" \
"v_lshlrev_b32 v106, 16, v131\n\t" \
"v_and_b32 v107, 0xffff0000, v131\n\t" \
"v_lshlrev_b32 v108, 16, v133\n\t" \
"v_and_b32 v109, 0xffff0000, v133\n\t" \
"v_lshlrev_b32 v110, 16, v135\n\t" \
"v_and_b32 v111, 0xffff0000, v135\n\t" \
"buffer_load_dwordx4 %[lb0], %[srd], s61 offen offset:2048 lds\n\t" \
"v_pk_add_f32 v[112:113], v[96:97], v[98:99]\n\t" \
"v_pk_add_f32 v[114:115], v[100:101], v[102:103]\n\t" \
"v_pk_add_f32 v[116:117], v[104:105], v[106:107]\n\t" \
"v_pk_add_f32 v[118:119], v[108:109], v[110:111]\n\t" \
"v_pk_add_f32 v[112:113], v[112:113], v[114:115]\n\t" \
"v_pk_add_f32 v[116:117], v[116:117], v[118:119]\n\t" \
"v_add_f32 v112, v112, v113\n\t" \
"v_add_f32 v113, v116, v117\n\t" \
"buffer_load_dwordx4 %[lb0], %[srd], s61 offen offset:3072 lds\n\t" \
"v_mov_b32 v114, v112\n\t" \
"v_mov_b32 v115, v113\n\t" \
"s_nop 0\n\t" \
"v_permlane16_swap_b32 v112, v114\n\t" \
"v_permlane16_swap_b32 v113, v115\n\t" \
"v_add_f32 v112, v112, v114\n\t" \
"v_add_f32 v113, v113, v115\n\t" \
"v_mov_b32 v114, v112\n\t" \
"v_mov_b32 v115, v113\n\t" \
"s_add_u32 s60, %[sX], 786432\n\t" \
"buffer_load_dwordx4 a[48:51], %[lb0], %[srd], s60 offen offset:0\n\t" \
"v_permlane32_swap_b32 v112, v114\n\t" \
"v_permlane32_swap_b32 v113, v115\n\t" \
"v_add_f32 v112, v112, v114\n\t" \
"v_add_f32 v113, v113, v115\n\t" \
"v_mul_f32 v120, 0x3d000000, v112\n\t" \
"v_mul_f32 v122, 0x3d000000, v113\n\t" \
"v_pk_add_f32 v[96:97], v[96:97], v[120:121] op_sel_hi:[1,0] neg_lo:[0,1] neg_hi:[0,1]\n\t" \
"v_pk_add_f32 v[98:99], v[98:99], v[120:121] op_sel_hi:[1,0] neg_lo:[0,1] neg_hi:[0,1]\n\t" \
"buffer_load_dwordx4 a[52:55], %[lb0], %[srd], s60 offen offset:1024\n\t" \
"v_pk_add_f32 v[100:101], v[100:101], v[120:121] op_sel_hi:[1,0] neg_lo:[0,1] neg_hi:[0,1]\n\t" \
"v_pk_add_f32 v[102:103], v[102:103], v[120:121] op_sel_hi:[1,0] neg_lo:[0,1] neg_hi:[0,1]\n\t" \
"v_pk_add_f32 v[104:105], v[104:105], v[122:123] op_sel_hi:[1,0] neg_lo:[0,1] neg_hi:[0,1]\n\t" \
"v_pk_add_f32 v[106:107], v[106:107], v[122:123] op_sel_hi:[1,0] neg_lo:[0,1] neg_hi:[0,1]\n\t" \
"v_pk_add_f32 v[108:109], v[108:109], v[122:123] op_sel_hi:[1,0] neg_lo:[0,1] neg_hi:[0,1]\n\t" \
"v_pk_add_f32 v[110:111], v[110:111], v[122:123] op_sel_hi:[1,0] neg_lo:[0,1] neg_hi:[0,1]\n\t" \
"v_pk_mul_f32 v[112:113], v[96:97], v[96:97]\n\t" \
"v_pk_mul_f32 v[114:115], v[104:105], v[104:105]\n\t" \
"s_add_u32 s61, %[sW7], 32768\n\t" \
"s_add_u32 m0, %[w7], 32768\n\t" \
"s_nop 0\n\t" \
"buffer_load_dwordx4 %[lb0], %[srd], s61 offen offset:0 lds\n\t" \
"v_pk_fma_f32 v[112:113], v[98:99], v[98:99], v[112:113]\n\t" \
"v_pk_fma_f32 v[114:115], v[106:107], v[106:107], v[114:115]\n\t" \
"v_pk_fma_f32 v[112:113], v[100:101], v[100:101], v[112:113]\n\t" \
"v_pk_fma_f32 v[114:115], v[108:109], v[108:109], v[114:115]\n\t" \
"v_pk_fma_f32 v[112:113], v[102:103], v[102:103], v[112:113]\n\t" \
"v_pk_fma_f32 v[114:115], v[110:111], v[110:111], v[114:115]\n\t" \
"v_add_f32 v121, v112, v113\n\t" \
"v_add_f32 v123, v114, v115\n\t" \
"buffer_load_dwordx4 %[lb0], %[srd], s61 offen offset:1024 lds\n\t" \
"v_mov_b32 v114, v121\n\t" \
"v_mov_b32 v115, v123\n\t" \
"s_nop 0\n\t" \
"v_permlane16_swap_b32 v121, v114\n\t" \
"v_permlane16_swap_b32 v123, v115\n\t" \
"v_add_f32 v121, v121, v114\n\t" \
"v_add_f32 v123, v123, v115\n\t" \
"v_mov_b32 v114, v121\n\t" \
"v_mov_b32 v115, v123\n\t" \
"buffer_load_dwordx4 %[lb0], %[srd], s61 offen offset:2048 lds\n\t" \
"v_permlane32_swap_b32 v121, v114\n\t" \
"v_permlane32_swap_b32 v123, v115\n\t" \
"v_add_f32 v121, v121, v114\n\t" \
"v_add_f32 v123, v123, v115\n\t" \
"v_and_b32 v121, 0x7fffffff, v121\n\t" \
"v_and_b32 v123, 0x7fffffff, v123\n\t" \
"s_mov_b32 exec_lo, 0xffff\n\t" \
"s_mov_b32 exec_hi, 0\n\t" \
"buffer_store_dwordx4 v[120:123], %[lb0], %[srd], %[sSGs] offen sc1\n\t" \
"s_mov_b64 exec, -1\n\t" \
"s_waitcnt vmcnt(21)\n\t" \
"s_barrier\n\t" \
"ds_read_b128 v[20:23], %[lb0] offset:0\n\t" \
"ds_read_b128 v[24:27], %[lb0] offset:1024\n\t" \
"ds_read_b128 v[28:31], %[lb0] offset:2048\n\t" \
"ds_read_b128 v[32:35], %[lb0] offset:3072\n\t" \
"ds_read_b128 v[36:39], %[lb0] offset:4096\n\t" \
"ds_read_b128 v[40:43], %[lb0] offset:5120\n\t" \
"v_accvgpr_read_b32 v144, a1\n\t" \
"v_accvgpr_read_b32 v145, a3\n\t" \
"v_accvgpr_mov_b32 a1, a4\n\t" \
"v_accvgpr_mov_b32 a3, a6\n\t" \
"v_accvgpr_write_b32 a4, v144\n\t" \
"v_accvgpr_write_b32 a6, v145\n\t" \
"s_waitcnt lgkmcnt(4)\n\t" \
"v_mfma_f32_16x16x32_bf16 v[192:195], v[20:23], a[0:3], 0\n\t" \
"v_accvgpr_read_b32 v146, a9\n\t" \
"v_accvgpr_read_b32 v147, a11\n\t" \
"v_mfma_f32_16x16x32_bf16 v[196:199], v[20:23], a[4:7], 0\n\t" \
"ds_read_b128 v[20:23], %[lb0] offset:6144\n\t" \
"v_accvgpr_mov_b32 a9, a12\n\t" \
"v_accvgpr_mov_b32 a11, a14\n\t" \
"v_mfma_f32_16x16x32_bf16 v[200:203], v[24:27], a[0:3], 0\n\t" \
"s_add_u32 s60, %[sX], 917504\n\t" \
"buffer_load_dwordx4 a[56:59], %[lb0], %[srd], s60 offen offset:0\n\t" \
"v_mfma_f32_16x16x32_bf16 v[204:207], v[24:27], a[4:7], 0\n\t" \
"ds_read_b128 v[24:27], %[lb0] offset:7168\n\t" \
"v_accvgpr_write_b32 a12, v146\n\t" \
"v_accvgpr_write_b32 a14, v147\n\t" \
"s_waitcnt lgkmcnt(4)\n\t" \
"v_mfma_f32_16x16x32_bf16 v[208:211], v[28:31], a[0:3], 0\n\t" \
"buffer_load_dwordx4 a[60:63], %[lb0], %[srd], s60 offen offset:1024\n\t" \
"v_mfma_f32_16x16x32_bf16 v[212:215], v[28:31], a[4:7], 0\n\t" \
"ds_read_b128 v[28:31], %[lb0] offset:8192\n\t" \
"v_mfma_f32_16x16x32_bf16 v[216:219], v[32:35], a[0:3], 0\n\t" \
"s_add_u32 s61, %[sW7], 57344\n\t" \
"s_add_u32 m0, %[w7], 57344\n\t" \
"s_nop 0\n\t" \
"buffer_load_dwordx4 %[lb0], %[srd], s61 offen offset:0 lds\n\t" \
"v_mfma_f32_16x16x32_bf16 v[220:223], v[32:35], a[4:7], 0\n\t" \
"ds_read_b128 v[32:35], %[lb0] offset:9216\n\t" \
"s_waitcnt lgkmcnt(4)\n\t" \
"v_mfma_f32_16x16x32_bf16 v[224:227], v[36:39], a[0:3], 0\n\t" \
"buffer_load_dwordx4 %[lb0], %[srd], s61 offen offset:1024 lds\n\t" \
"v_mfma_f32_16x16x32_bf16 v[228:231], v[36:39], a[4:7], 0\n\t" \
"ds_read_b128 v[36:39], %[lb0] offset:10240\n\t" \
"v_mfma_f32_16x16x32_bf16 v[232:235], v[40:43], a[0:3], 0\n\t" \
"v_mfma_f32_16x16x32_bf16 v[236:239], v[40:43], a[4:7], 0\n\t" \
"ds_read_b128 v[40:43], %[lb0] offset:11264\n\t" \
"s_waitcnt lgkmcnt(4)\n\t" \
"v_mfma_f32_16x16x32_bf16 v[240:243], v[20:23], a[0:3], 0\n\t" \
"s_add_u32 s60, %[sX], 1048576\n\t" \
"buffer_load_dwordx4 a[64:67], %[lb0], %[srd], s60 offen offset:0\n\t" \
"v_mfma_f32_16x16x32_bf16 v[244:247], v[20:23], a[4:7], 0\n\t" \
"ds_read_b128 v[20:23], %[lb0] offset:12288\n\t" \
"v_mfma_f32_16x16x32_bf16 v[248:251], v[24:27], a[0:3], 0\n\t" \
"buffer_load_dwordx4 a[68:71], %[lb0], %[srd], s60 offen offset:1024\n\t" \
"v_mfma_f32_16x16x32_bf16 v[252:255], v[24:27], a[4:7], 0\n\t" \
"ds_read_b128 v[24:27], %[lb0] offset:13312\n\t" \
"s_waitcnt lgkmcnt(4)\n\t" \
"v_mfma_f32_16x16x32_bf16 v[192:195], v[28:31], a[8:11], v[192:195]\n\t" \
"buffer_load_dwordx4 %[lb0], %[srd], s61 offen offset:2048 lds\n\t" \
"v_mfma_f32_16x16x32_bf16 v[196:199], v[28:31], a[12:15], v[196:199]\n\t" \
"ds_read_b128 v[28:31], %[lb0] offset:14336\n\t" \
"v_mfma_f32_16x16x32_bf16 v[200:203], v[32:35], a[8:11], v[200:203]\n\t" \
"buffer_load_dwordx4 %[lb0], %[srd], s61 offen offset:3072 lds\n\t" \
"v_mfma_f32_16x16x32_bf16 v[204:207], v[32:35], a[12:15], v[204:207]\n\t" \
"ds_read_b128 v[32:35], %[lb0] offset:15360\n\t" \
"s_waitcnt lgkmcnt(4)\n\t" \
"v_mfma_f32_16x16x32_bf16 v[208:211], v[36:39], a[8:11], v[208:211]\n\t" \
"v_mfma_f32_16x16x32_bf16 v[212:215], v[36:39], a[12:15], v[212:215]\n\t" \
"s_waitcnt vmcnt(22)\n\t" \
"s_barrier\n\t" \
"ds_read_b128 v[36:39], %[lb0] offset:16384\n\t" \
"v_accvgpr_read_b32 v148, a17\n\t" \
"v_accvgpr_read_b32 v149, a19\n\t" \
"v_accvgpr_mov_b32 a17, a20\n\t" \
"v_accvgpr_mov_b32 a19, a22\n\t" \
"v_accvgpr_write_b32 a20, v148\n\t" \
"v_mfma_f32_16x16x32_bf16 v[216:219], v[40:43], a[8:11], v[216:219]\n\t" \
"s_add_u32 s60, %[sX], 1179648\n\t" \
"buffer_load_dwordx4 a[72:75], %[lb0], %[srd], s60 offen offset:0\n\t" \
"v_mfma_f32_16x16x32_bf16 v[220:223], v[40:43], a[12:15], v[220:223]\n\t" \
"ds_read_b128 v[40:43], %[lb0] offset:17408\n\t" \
"v_accvgpr_write_b32 a22, v149\n\t" \
"v_accvgpr_read_b32 v150, a25\n\t" \
"v_accvgpr_read_b32 v151, a27\n\t" \
"s_waitcnt lgkmcnt(4)\n\t" \
"v_mfma_f32_16x16x32_bf16 v[224:227], v[20:23], a[8:11], v[224:227]\n\t" \
"buffer_load_dwordx4 a[76:79], %[lb0], %[srd], s60 offen offset:1024\n\t" \
"v_mfma_f32_16x16x32_bf16 v[228:231], v[20:23], a[12:15], v[228:231]\n\t" \
"ds_read_b128 v[20:23], %[lb0] offset:18432\n\t" \
"v_accvgpr_mov_b32 a25, a28\n\t" \
"v_accvgpr_mov_b32 a27, a30\n\t" \
"v_accvgpr_write_b32 a28, v150\n\t" \
"v_mfma_f32_16x16x32_bf16 v[232:235], v[24:27], a[8:11], v[232:235]\n\t" \
"s_add_u32 s61, %[sW7], 61440\n\t" \
"s_add_u32 m0, %[w7], 61440\n\t" \
"s_nop 0\n\t" \
"buffer_load_dwordx4 %[lb0], %[srd], s61 offen offset:0 lds\n\t" \
"v_mfma_f32_16x16x32_bf16 v[236:239], v[24:27], a[12:15], v[236:239]\n\t" \
"ds_read_b128 v[24:27], %[lb0] offset:19456\n\t" \
"v_accvgpr_write_b32 a30, v151\n\t" \
"s_waitcnt lgkmcnt(4)\n\t" \
"v_mfma_f32_16x16x32_bf16 v[240:243], v[28:31], a[8:11], v[240:243]\n\t" \
"buffer_load_dwordx4 %[lb0], %[srd], s61 offen offset:1024 lds\n\t" \
"v_mfma_f32_16x16x32_bf16 v[244:247], v[28:31], a[12:15], v[244:247]\n\t" \
"ds_read_b128 v[28:31], %[lb0] offset:20480\n\t" \
"v_mfma_f32_16x16x32_bf16 v[248:251], v[32:35], a[8:11], v[248:251]\n\t" \
"v_mfma_f32_16x16x32_bf16 v[252:255], v[32:35], a[12:15], v[252:255]\n\t" \
"ds_read_b128 v[32:35], %[lb0] offset:21504\n\t" \
"s_waitcnt lgkmcnt(4)\n\t" \
"v_mfma_f32_16x16x32_bf16 v[192:195], v[36:39], a[16:19], v[192:195]\n\t" \
"s_add_u32 s60, %[sX], 1310720\n\t" \
"buffer_load_dwordx4 a[80:83], %[lb0], %[srd], s60 offen offset:0\n\t" \
"v_mfma_f32_16x16x32_bf16 v[196:199], v[36:39], a[20:23], v[196:199]\n\t" \
"ds_read_b128 v[36:39], %[lb0] offset:22528\n\t" \
"v_mfma_f32_16x16x32_bf16 v[200:203], v[40:43], a[16:19], v[200:203]\n\t" \
"buffer_load_dwordx4 a[84:87], %[lb0], %[srd], s60 offen offset:1024\n\t" \
"v_mfma_f32_16x16x32_bf16 v[204:207], v[40:43], a[20:23], v[204:207]\n\t" \
"ds_read_b128 v[40:43], %[lb0] offset:23552\n\t" \
"s_waitcnt lgkmcnt(4)\n\t" \
"v_mfma_f32_16x16x32_bf16 v[208:211], v[20:23], a[16:19], v[208:211]\n\t" \
"buffer_load_dwordx4 %[lb0], %[srd], s61 offen offset:2048 lds\n\t" \
"v_mfma_f32_16x16x32_bf16 v[212:215], v[20:23], a[20:23], v[212:215]\n\t" \
"ds_read_b128 v[20:23], %[lb0] offset:24576\n\t" \
"v_mfma_f32_16x16x32_bf16 v[216:219], v[24:27], a[16:19], v[216:219]\n\t" \
"v_mfma_f32_16x16x32_bf16 v[220:223], v[24:27], a[20:23], v[220:223]\n\t" \
"ds_read_b128 v[24:27], %[lb0] offset:25600\n\t" \
"s_waitcnt lgkmcnt(4)\n\t" \
"v_mfma_f32_16x16x32_bf16 v[224:227], v[28:31], a[16:19], v[224:227]\n\t" \
"v_mfma_f32_16x16x32_bf16 v[228:231], v[28:31], a[20:23], v[228:231]\n\t" \
"ds_read_b128 v[28:31], %[lb0] offset:26624\n\t" \
"v_mfma_f32_16x16x32_bf16 v[232:235], v[32:35], a[16:19], v[232:235]\n\t" \
"v_mfma_f32_16x16x32_bf16 v[236:239], v[32:35], a[20:23], v[236:239]\n\t" \
"ds_read_b128 v[32:35], %[lb0] offset:27648\n\t" \
"s_waitcnt lgkmcnt(4)\n\t" \
"v_mfma_f32_16x16x32_bf16 v[240:243], v[36:39], a[16:19], v[240:243]\n\t" \
"v_mfma_f32_16x16x32_bf16 v[244:247], v[36:39], a[20:23], v[244:247]\n\t" \
"s_waitcnt vmcnt(16)\n\t" \
"s_barrier\n\t" \
"ds_read_b128 v[36:39], %[lb0] offset:28672\n\t" \
"v_accvgpr_read_b32 v152, a33\n\t" \
"v_accvgpr_read_b32 v153, a35\n\t" \
"v_accvgpr_mov_b32 a33, a36\n\t" \
"v_accvgpr_mov_b32 a35, a38\n\t" \
"v_accvgpr_write_b32 a36, v152\n\t" \
"v_mfma_f32_16x16x32_bf16 v[248:251], v[40:43], a[16:19], v[248:251]\n\t" \
"v_accvgpr_write_b32 a38, v153\n\t" \
"v_accvgpr_read_b32 v154, a41\n\t" \
"v_mfma_f32_16x16x32_bf16 v[252:255], v[40:43], a[20:23], v[252:255]\n\t" \
"ds_read_b128 v[40:43], %[lb0] offset:29696\n\t" \
"v_accvgpr_read_b32 v155, a43\n\t" \
"v_accvgpr_mov_b32 a41, a44\n\t" \
"s_waitcnt lgkmcnt(4)\n\t" \
"v_mfma_f32_16x16x32_bf16 v[192:195], v[20:23], a[24:27], v[192:195]\n\t" \
"v_accvgpr_mov_b32 a43, a46\n\t" \
"v_accvgpr_write_b32 a44, v154\n\t" \
"v_mfma_f32_16x16x32_bf16 v[196:199], v[20:23], a[28:31], v[196:199]\n\t" \
"ds_read_b128 v[20:23], %[lb0] offset:30720\n\t" \
"v_accvgpr_write_b32 a46, v155\n\t" \
"v_accvgpr_read_b32 v156, a49\n\t" \
"v_mfma_f32_16x16x32_bf16 v[200:203], v[24:27], a[24:27], v[200:203]\n\t" \
"v_accvgpr_read_b32 v157, a51\n\t" \
"v_accvgpr_mov_b32 a49, a52\n\t" \
"v_mfma_f32_16x16x32_bf16 v[204:207], v[24:27], a[28:31], v[204:207]\n\t" \
"ds_read_b128 v[24:27], %[lb0] offset:31744\n\t" \
"v_accvgpr_mov_b32 a51, a54\n\t" \
"v_accvgpr_write_b32 a52, v156\n\t" \
"s_waitcnt lgkmcnt(4)\n\t" \
"v_mfma_f32_16x16x32_bf16 v[208:211], v[28:31], a[24:27], v[208:211]\n\t" \
"v_accvgpr_write_b32 a54, v157\n\t" \
"v_mfma_f32_16x16x32_bf16 v[212:215], v[28:31], a[28:31], v[212:215]\n\t" \
"ds_read_b128 v[28:31], %[lb0] offset:32768\n\t" \
"v_mfma_f32_16x16x32_bf16 v[216:219], v[32:35], a[24:27], v[216:219]\n\t" \
"v_mfma_f32_16x16x32_bf16 v[220:223], v[32:35], a[28:31], v[220:223]\n\t" \
"ds_read_b128 v[32:35], %[lb0] offset:33792\n\t" \
"s_waitcnt lgkmcnt(4)\n\t" \
"v_mfma_f32_16x16x32_bf16 v[224:227], v[36:39], a[24:27], v[224:227]\n\t" \
"s_add_u32 s60, %[sX], 1441792\n\t" \
"buffer_load_dwordx4 a[88:91], %[lb0], %[srd], s60 offen offset:0\n\t" \
"v_mfma_f32_16x16x32_bf16 v[228:231], v[36:39], a[28:31], v[228:231]\n\t" \
"ds_read_b128 v[36:39], %[lb0] offset:34816\n\t" \
"v_mfma_f32_16x16x32_bf16 v[232:235], v[40:43], a[24:27], v[232:235]\n\t" \
"buffer_load_dwordx4 a[92:95], %[lb0], %[srd], s60 offen offset:1024\n\t" \
"v_mfma_f32_16x16x32_bf16 v[236:239], v[40:43], a[28:31], v[236:239]\n\t" \
"ds_read_b128 v[40:43], %[lb0] offset:35840\n\t" \
"s_waitcnt lgkmcnt(4)\n\t" \
"v_mfma_f32_16x16x32_bf16 v[240:243], v[20:23], a[24:27], v[240:243]\n\t" \
"v_mfma_f32_16x16x32_bf16 v[244:247], v[20:23], a[28:31], v[244:247]\n\t" \
"ds_read_b128 v[20:23], %[lb0] offset:36864\n\t" \
"v_mfma_f32_16x16x32_bf16 v[248:251], v[24:27], a[24:27], v[248:251]\n\t" \
"s_add_u32 s61, %[sW7], 86016\n\t" \
"s_add_u32 m0, %[w7], 86016\n\t" \
"s_nop 0\n\t" \
"buffer_load_dwordx4 %[lb0], %[srd], s61 offen offset:0 lds\n\t" \
"v_mfma_f32_16x16x32_bf16 v[252:255], v[24:27], a[28:31], v[252:255]\n\t" \
"ds_read_b128 v[24:27], %[lb0] offset:37888\n\t" \
"s_waitcnt lgkmcnt(4)\n\t" \
"v_mfma_f32_16x16x32_bf16 v[192:195], v[28:31], a[32:35], v[192:195]\n\t" \
"buffer_load_dwordx4 %[lb0], %[srd], s61 offen offset:1024 lds\n\t" \
"v_mfma_f32_16x16x32_bf16 v[196:199], v[28:31], a[36:39], v[196:199]\n\t" \
"ds_read_b128 v[28:31], %[lb0] offset:38912\n\t" \
"v_mfma_f32_16x16x32_bf16 v[200:203], v[32:35], a[32:35], v[200:203]\n\t" \
"v_mfma_f32_16x16x32_bf16 v[204:207], v[32:35], a[36:39], v[204:207]\n\t" \
"ds_read_b128 v[32:35], %[lb0] offset:39936\n\t" \
"s_waitcnt lgkmcnt(4)\n\t" \
"v_mfma_f32_16x16x32_bf16 v[208:211], v[36:39], a[32:35], v[208:211]\n\t" \
"s_add_u32 s60, %[sX], 1572864\n\t" \
"buffer_load_dwordx4 a[96:99], %[lb0], %[srd], s60 offen offset:0\n\t" \
"v_mfma_f32_16x16x32_bf16 v[212:215], v[36:39], a[36:39], v[212:215]\n\t" \
"ds_read_b128 v[36:39], %[lb0] offset:40960\n\t" \
"v_mfma_f32_16x16x32_bf16 v[216:219], v[40:43], a[32:35], v[216:219]\n\t" \
"buffer_load_dwordx4 a[100:103], %[lb0], %[srd], s60 offen offset:1024\n\t" \
"v_mfma_f32_16x16x32_bf16 v[220:223], v[40:43], a[36:39], v[220:223]\n\t" \
"ds_read_b128 v[40:43], %[lb0] offset:41984\n\t" \
"s_waitcnt lgkmcnt(4)\n\t" \
"v_mfma_f32_16x16x32_bf16 v[224:227], v[20:23], a[32:35], v[224:227]\n\t" \
"v_mfma_f32_16x16x32_bf16 v[228:231], v[20:23], a[36:39], v[228:231]\n\t" \
"ds_read_b128 v[20:23], %[lb0] offset:43008\n\t" \
"v_mfma_f32_16x16x32_bf16 v[232:235], v[24:27], a[32:35], v[232:235]\n\t" \
"buffer_load_dwordx4 %[lb0], %[srd], s61 offen offset:2048 lds\n\t" \
"v_mfma_f32_16x16x32_bf16 v[236:239], v[24:27], a[36:39], v[236:239]\n\t" \
"ds_read_b128 v[24:27], %[lb0] offset:44032\n\t" \
"s_waitcnt lgkmcnt(4)\n\t" \
"v_mfma_f32_16x16x32_bf16 v[240:243], v[28:31], a[32:35], v[240:243]\n\t" \
"buffer_load_dwordx4 %[lb0], %[srd], s61 offen offset:3072 lds\n\t" \
"v_mfma_f32_16x16x32_bf16 v[244:247], v[28:31], a[36:39], v[244:247]\n\t" \
"ds_read_b128 v[28:31], %[lb0] offset:45056\n\t" \
"v_mfma_f32_16x16x32_bf16 v[248:251], v[32:35], a[32:35], v[248:251]\n\t" \
"v_mfma_f32_16x16x32_bf16 v[252:255], v[32:35], a[36:39], v[252:255]\n\t" \
"ds_read_b128 v[32:35], %[lb0] offset:46080\n\t" \
"s_waitcnt lgkmcnt(4)\n\t" \
"v_mfma_f32_16x16x32_bf16 v[192:195], v[36:39], a[40:43], v[192:195]\n\t" \
"s_add_u32 s60, %[sX], 1703936\n\t" \
"buffer_load_dwordx4 a[104:107], %[lb0], %[srd], s60 offen offset:0\n\t" \
"v_mfma_f32_16x16x32_bf16 v[196:199], v[36:39], a[44:47], v[196:199]\n\t" \
"ds_read_b128 v[36:39], %[lb0] offset:47104\n\t" \
"v_mfma_f32_16x16x32_bf16 v[200:203], v[40:43], a[40:43], v[200:203]\n\t" \
"buffer_load_dwordx4 a[108:111], %[lb0], %[srd], s60 offen offset:1024\n\t" \
"v_mfma_f32_16x16x32_bf16 v[204:207], v[40:43], a[44:47], v[204:207]\n\t" \
"ds_read_b128 v[40:43], %[lb0] offset:48128\n\t" \
"s_waitcnt lgkmcnt(4)\n\t" \
"v_mfma_f32_16x16x32_bf16 v[208:211], v[20:23], a[40:43], v[208:211]\n\t" \
"v_mfma_f32_16x16x32_bf16 v[212:215], v[20:23], a[44:47], v[212:215]\n\t" \
"ds_read_b128 v[20:23], %[lb0] offset:49152\n\t" \
"v_mfma_f32_16x16x32_bf16 v[216:219], v[24:27], a[40:43], v[216:219]\n\t" \
"s_add_u32 s61, %[sW7], 90112\n\t" \
"s_add_u32 m0, %[w7], 90112\n\t" \
"s_nop 0\n\t" \
"buffer_load_dwordx4 %[lb0], %[srd], s61 offen offset:0 lds\n\t" \
"v_mfma_f32_16x16x32_bf16 v[220:223], v[24:27], a[44:47], v[220:223]\n\t" \
"ds_read_b128 v[24:27], %[lb0] offset:50176\n\t" \
"s_waitcnt lgkmcnt(4)\n\t" \
"v_mfma_f32_16x16x32_bf16 v[224:227], v[28:31], a[40:43], v[224:227]\n\t" \
"buffer_load_dwordx4 v[128:131], %[sgv], %[srd], %[sSGl] offen offset:0 sc1\n\t" \
"v_mfma_f32_16x16x32_bf16 v[228:231], v[28:31], a[44:47], v[228:231]\n\t" \
"ds_read_b128 v[28:31], %[lb0] offset:51200\n\t" \
"v_mfma_f32_16x16x32_bf16 v[232:235], v[32:35], a[40:43], v[232:235]\n\t" \
"buffer_load_dwordx4 v[132:135], %[sgv], %[srd], %[sSGl] offen offset:1024 sc1\n\t" \
"v_mfma_f32_16x16x32_bf16 v[236:239], v[32:35], a[44:47], v[236:239]\n\t" \
"ds_read_b128 v[32:35], %[lb0] offset:52224\n\t" \
"s_waitcnt lgkmcnt(4)\n\t" \
"v_mfma_f32_16x16x32_bf16 v[240:243], v[36:39], a[40:43], v[240:243]\n\t" \
"buffer_load_dwordx4 v[136:139], %[sgv], %[srd], %[sSGl] offen offset:2048 sc1\n\t" \
"v_mfma_f32_16x16x32_bf16 v[244:247], v[36:39], a[44:47], v[244:247]\n\t" \
"ds_read_b128 v[36:39], %[lb0] offset:53248\n\t" \
"v_mfma_f32_16x16x32_bf16 v[248:251], v[40:43], a[40:43], v[248:251]\n\t" \
"buffer_load_dwordx4 v[140:143], %[sgv], %[srd], %[sSGl] offen offset:3072 sc1\n\t" \
"v_mfma_f32_16x16x32_bf16 v[252:255], v[40:43], a[44:47], v[252:255]\n\t" \
"ds_read_b128 v[40:43], %[lb0] offset:54272\n\t" \
"s_waitcnt lgkmcnt(4)\n\t" \
"v_mfma_f32_16x16x32_bf16 v[192:195], v[20:23], a[48:51], v[192:195]\n\t" \
"buffer_load_dwordx4 %[lb0], %[srd], s61 offen offset:1024 lds\n\t" \
"v_mfma_f32_16x16x32_bf16 v[196:199], v[20:23], a[52:55], v[196:199]\n\t" \
"ds_read_b128 v[20:23], %[lb0] offset:55296\n\t" \
"v_mfma_f32_16x16x32_bf16 v[200:203], v[24:27], a[48:51], v[200:203]\n\t" \
"buffer_load_dwordx4 %[lb0], %[srd], s61 offen offset:2048 lds\n\t" \
"v_mfma_f32_16x16x32_bf16 v[204:207], v[24:27], a[52:55], v[204:207]\n\t" \
"ds_read_b128 v[24:27], %[lb0] offset:56320\n\t" \
"s_waitcnt lgkmcnt(4)\n\t" \
"v_mfma_f32_16x16x32_bf16 v[208:211], v[28:31], a[48:51], v[208:211]\n\t" \
"v_mfma_f32_16x16x32_bf16 v[212:215], v[28:31], a[52:55], v[212:215]\n\t" \
"s_waitcnt vmcnt(17)\n\t" \
"s_barrier\n\t" \
"ds_read_b128 v[28:31], %[lb1] offset:0\n\t" \
"v_accvgpr_read_b32 v158, a57\n\t" \
"v_accvgpr_read_b32 v159, a59\n\t" \
"v_accvgpr_mov_b32 a57, a60\n\t" \
"v_accvgpr_mov_b32 a59, a62\n\t" \
"v_accvgpr_write_b32 a60, v158\n\t" \
"v_mfma_f32_16x16x32_bf16 v[216:219], v[32:35], a[48:51], v[216:219]\n\t" \
"v_accvgpr_write_b32 a62, v159\n\t" \
"v_accvgpr_read_b32 v144, a65\n\t" \
"v_mfma_f32_16x16x32_bf16 v[220:223], v[32:35], a[52:55], v[220:223]\n\t" \
"ds_read_b128 v[32:35], %[lb1] offset:1024\n\t" \
"v_accvgpr_read_b32 v145, a67\n\t" \
"v_accvgpr_mov_b32 a65, a68\n\t" \
"s_waitcnt lgkmcnt(4)\n\t" \
"v_mfma_f32_16x16x32_bf16 v[224:227], v[36:39], a[48:51], v[224:227]\n\t" \
"v_accvgpr_mov_b32 a67, a70\n\t" \
"v_accvgpr_write_b32 a68, v144\n\t" \
"v_mfma_f32_16x16x32_bf16 v[228:231], v[36:39], a[52:55], v[228:231]\n\t" \
"ds_read_b128 v[36:39], %[lb1] offset:2048\n\t" \
"v_accvgpr_write_b32 a70, v145\n\t" \
"v_accvgpr_read_b32 v146, a73\n\t" \
"v_mfma_f32_16x16x32_bf16 v[232:235], v[40:43], a[48:51], v[232:235]\n\t" \
"v_accvgpr_read_b32 v147, a75\n\t" \
"v_accvgpr_mov_b32 a73, a76\n\t" \
"v_mfma_f32_16x16x32_bf16 v[236:239], v[40:43], a[52:55], v[236:239]\n\t" \
"ds_read_b128 v[40:43], %[lb1] offset:3072\n\t" \
"v_accvgpr_mov_b32 a75, a78\n\t" \
"v_accvgpr_write_b32 a76, v146\n\t" \
"s_waitcnt lgkmcnt(4)\n\t" \
"v_mfma_f32_16x16x32_bf16 v[240:243], v[20:23], a[48:51], v[240:243]\n\t" \
"v_accvgpr_write_b32 a78, v147\n\t" \
"v_accvgpr_read_b32 v148, a81\n\t" \
"v_mfma_f32_16x16x32_bf16 v[244:247], v[20:23], a[52:55], v[244:247]\n\t" \
"ds_read_b128 v[20:23], %[lb1] offset:4096\n\t" \
"v_accvgpr_read_b32 v149, a83\n\t" \
"v_accvgpr_mov_b32 a81, a84\n\t" \
"v_mfma_f32_16x16x32_bf16 v[248:251], v[24:27], a[48:51], v[248:251]\n\t" \
"v_accvgpr_mov_b32 a83, a86\n\t" \
"v_accvgpr_write_b32 a84, v148\n\t" \
"v_mfma_f32_16x16x32_bf16 v[252:255], v[24:27], a[52:55], v[252:255]\n\t" \
"ds_read_b128 v[24:27], %[lb1] offset:5120\n\t" \
"v_accvgpr_write_b32 a86, v149\n\t" \
"s_waitcnt lgkmcnt(4)\n\t" \
"v_mfma_f32_16x16x32_bf16 v[192:195], v[28:31], a[56:59], v[192:195]\n\t" \
"s_add_u32 s60, %[sX], 1835008\n\t" \
"buffer_load_dwordx4 a[112:115], %[lb0], %[srd], s60 offen offset:0\n\t" \
"v_mfma_f32_16x16x32_bf16 v[196:199], v[28:31], a[60:63], v[196:199]\n\t" \
"ds_read_b128 v[28:31], %[lb1] offset:6144\n\t" \
"v_mfma_f32_16x16x32_bf16 v[200:203], v[32:35], a[56:59], v[200:203]\n\t" \
"buffer_load_dwordx4 a[116:119], %[lb0], %[srd], s60 offen offset:1024\n\t" \
"v_mfma_f32_16x16x32_bf16 v[204:207], v[32:35], a[60:63], v[204:207]\n\t" \
"ds_read_b128 v[32:35], %[lb1] offset:7168\n\t" \
"s_waitcnt lgkmcnt(4)\n\t" \
"v_mfma_f32_16x16x32_bf16 v[208:211], v[36:39], a[56:59], v[208:211]\n\t" \
"v_mfma_f32_16x16x32_bf16 v[212:215], v[36:39], a[60:63], v[212:215]\n\t" \
"ds_read_b128 v[36:39], %[lb1] offset:8192\n\t" \
"v_mfma_f32_16x16x32_bf16 v[216:219], v[40:43], a[56:59], v[216:219]\n\t" \
"s_add_u32 s61, %[sW7], 114688\n\t" \
"s_add_u32 m0, %[w7], 114688\n\t" \
"s_nop 0\n\t" \
"buffer_load_dwordx4 %[lb0], %[srd], s61 offen offset:0 lds\n\t" \
"v_mfma_f32_16x16x32_bf16 v[220:223], v[40:43], a[60:63], v[220:223]\n\t" \
"ds_read_b128 v[40:43], %[lb1] offset:9216\n\t" \
"s_waitcnt lgkmcnt(4)\n\t" \
"v_mfma_f32_16x16x32_bf16 v[224:227], v[20:23], a[56:59], v[224:227]\n\t" \
"buffer_load_dwordx4 %[lb0], %[srd], s61 offen offset:1024 lds\n\t" \
"v_mfma_f32_16x16x32_bf16 v[228:231], v[20:23], a[60:63], v[228:231]\n\t" \
"ds_read_b128 v[20:23], %[lb1] offset:10240\n\t" \
"v_mfma_f32_16x16x32_bf16 v[232:235], v[24:27], a[56:59], v[232:235]\n\t" \
"v_mfma_f32_16x16x32_bf16 v[236:239], v[24:27], a[60:63], v[236:239]\n\t" \
"ds_read_b128 v[24:27], %[lb1] offset:11264\n\t" \
"s_waitcnt lgkmcnt(4)\n\t" \
"v_mfma_f32_16x16x32_bf16 v[240:243], v[28:31], a[56:59], v[240:243]\n\t" \
"s_add_u32 s60, %[sX], 1966080\n\t" \
"buffer_load_dwordx4 a[120:123], %[lb0], %[srd], s60 offen offset:0\n\t" \
"v_mfma_f32_16x16x32_bf16 v[244:247], v[28:31], a[60:63], v[244:247]\n\t" \
"ds_read_b128 v[28:31], %[lb1] offset:12288\n\t" \
"v_mfma_f32_16x16x32_bf16 v[248:251], v[32:35], a[56:59], v[248:251]\n\t" \
"buffer_load_dwordx4 a[124:127], %[lb0], %[srd], s60 offen offset:1024\n\t" \
"v_mfma_f32_16x16x32_bf16 v[252:255], v[32:35], a[60:63], v[252:255]\n\t" \
"ds_read_b128 v[32:35], %[lb1] offset:13312\n\t" \
"s_waitcnt lgkmcnt(4)\n\t" \
"v_mfma_f32_16x16x32_bf16 v[192:195], v[36:39], a[64:67], v[192:195]\n\t" \
"v_mfma_f32_16x16x32_bf16 v[196:199], v[36:39], a[68:71], v[196:199]\n\t" \
"ds_read_b128 v[36:39], %[lb1] offset:14336\n\t" \
"v_mfma_f32_16x16x32_bf16 v[200:203], v[40:43], a[64:67], v[200:203]\n\t" \
"buffer_load_dwordx4 %[lb0], %[srd], s61 offen offset:2048 lds\n\t" \
"v_mfma_f32_16x16x32_bf16 v[204:207], v[40:43], a[68:71], v[204:207]\n\t" \
"ds_read_b128 v[40:43], %[lb1] offset:15360\n\t" \
"s_waitcnt lgkmcnt(4)\n\t" \
"v_mfma_f32_16x16x32_bf16 v[208:211], v[20:23], a[64:67], v[208:211]\n\t" \
"buffer_load_dwordx4 %[lb0], %[srd], s61 offen offset:3072 lds\n\t" \
"v_mfma_f32_16x16x32_bf16 v[212:215], v[20:23], a[68:71], v[212:215]\n\t" \
"ds_read_b128 v[20:23], %[lb1] offset:16384\n\t" \
"v_mfma_f32_16x16x32_bf16 v[216:219], v[24:27], a[64:67], v[216:219]\n\t" \
"v_mfma_f32_16x16x32_bf16 v[220:223], v[24:27], a[68:71], v[220:223]\n\t" \
"ds_read_b128 v[24:27], %[lb1] offset:17408\n\t" \
"s_waitcnt lgkmcnt(4)\n\t" \
"v_mfma_f32_16x16x32_bf16 v[224:227], v[28:31], a[64:67], v[224:227]\n\t" \
"s_add_u32 s61, %[sW7], 118784\n\t" \
"s_add_u32 m0, %[w7], 118784\n\t" \
"s_nop 0\n\t" \
"buffer_load_dwordx4 %[lb0], %[srd], s61 offen offset:0 lds\n\t" \
"v_mfma_f32_16x16x32_bf16 v[228:231], v[28:31], a[68:71], v[228:231]\n\t" \
"ds_read_b128 v[28:31], %[lb1] offset:18432\n\t" \
"v_mfma_f32_16x16x32_bf16 v[232:235], v[32:35], a[64:67], v[232:235]\n\t" \
"buffer_load_dwordx4 %[lb0], %[srd], s61 offen offset:1024 lds\n\t" \
"v_mfma_f32_16x16x32_bf16 v[236:239], v[32:35], a[68:71], v[236:239]\n\t" \
"ds_read_b128 v[32:35], %[lb1] offset:19456\n\t" \
"s_waitcnt lgkmcnt(4)\n\t" \
"v_mfma_f32_16x16x32_bf16 v[240:243], v[36:39], a[64:67], v[240:243]\n\t" \
"v_mfma_f32_16x16x32_bf16 v[244:247], v[36:39], a[68:71], v[244:247]\n\t" \
"ds_read_b128 v[36:39], %[lb1] offset:20480\n\t" \
"v_mfma_f32_16x16x32_bf16 v[248:251], v[40:43], a[64:67], v[248:251]\n\t" \
"buffer_load_dwordx4 %[lb0], %[srd], s61 offen offset:2048 lds\n\t" \
"v_mfma_f32_16x16x32_bf16 v[252:255], v[40:43], a[68:71], v[252:255]\n\t" \
"ds_read_b128 v[40:43], %[lb1] offset:21504\n\t" \
"s_waitcnt lgkmcnt(4)\n\t" \
"v_mfma_f32_16x16x32_bf16 v[192:195], v[20:23], a[72:75], v[192:195]\n\t" \
"s_add_u32 s61, %[sW1], 229376\n\t" \
"s_add_u32 m0, %[w1], 143360\n\t" \
"s_nop 0\n\t" \
"buffer_load_dwordx4 %[lb0], %[srd], s61 offen lds\n\t" \
"v_mfma_f32_16x16x32_bf16 v[196:199], v[20:23], a[76:79], v[196:199]\n\t" \
"ds_read_b128 v[20:23], %[lb1] offset:22528\n\t" \
"v_mfma_f32_16x16x32_bf16 v[200:203], v[24:27], a[72:75], v[200:203]\n\t" \
"v_mfma_f32_16x16x32_bf16 v[204:207], v[24:27], a[76:79], v[204:207]\n\t" \
"ds_read_b128 v[24:27], %[lb1] offset:23552\n\t" \
"s_waitcnt lgkmcnt(4)\n\t" \
"v_mfma_f32_16x16x32_bf16 v[208:211], v[28:31], a[72:75], v[208:211]\n\t" \
"s_add_u32 s61, %[sW1], 233472\n\t" \
"s_add_u32 m0, %[w1], 147456\n\t" \
"s_nop 0\n\t" \
"buffer_load_dwordx4 %[lb0], %[srd], s61 offen lds\n\t" \
"v_mfma_f32_16x16x32_bf16 v[212:215], v[28:31], a[76:79], v[212:215]\n\t" \
"ds_read_b128 v[28:31], %[lb1] offset:24576\n\t" \
"v_mfma_f32_16x16x32_bf16 v[216:219], v[32:35], a[72:75], v[216:219]\n\t" \
"v_mfma_f32_16x16x32_bf16 v[220:223], v[32:35], a[76:79], v[220:223]\n\t" \
"ds_read_b128 v[32:35], %[lb1] offset:25600\n\t" \
"s_waitcnt lgkmcnt(4)\n\t" \
"v_mfma_f32_16x16x32_bf16 v[224:227], v[36:39], a[72:75], v[224:227]\n\t" \
"s_waitcnt vmcnt(15)\n\t" \
"Lchk%=:\n\t" \
"v_or3_b32 v12, v129, v131, v133\n\t" \
"v_or3_b32 v13, v135, v137, v139\n\t" \
"v_or3_b32 v12, v12, v141, v143\n\t" \
"v_or_b32 v12, v12, v13\n\t" \
"v_cmp_gt_i32 vcc, 0, v12\n\t" \
"s_cbranch_vccnz Lre%=\n\t" \
"Lok%=:\n\t" \
"v_mfma_f32_16x16x32_bf16 v[228:231], v[36:39], a[76:79], v[228:231]\n\t" \
"ds_read_b128 v[36:39], %[lb1] offset:26624\n\t" \
"v_mfma_f32_16x16x32_bf16 v[232:235], v[40:43], a[72:75], v[232:235]\n\t" \
"v_add_f32 v12, v128, v132\n\t" \
"v_add_f32 v14, v136, v140\n\t" \
"v_mfma_f32_16x16x32_bf16 v[236:239], v[40:43], a[76:79], v[236:239]\n\t" \
"ds_read_b128 v[40:43], %[lb1] offset:27648\n\t" \
"s_waitcnt lgkmcnt(4)\n\t" \
"v_mfma_f32_16x16x32_bf16 v[240:243], v[20:23], a[72:75], v[240:243]\n\t" \
"v_add_f32 v13, v130, v134\n\t" \
"v_add_f32 v15, v138, v142\n\t" \
"v_mfma_f32_16x16x32_bf16 v[244:247], v[20:23], a[76:79], v[244:247]\n\t" \
"s_waitcnt vmcnt(13)\n\t" \
"s_barrier\n\t" \
"ds_read_b128 v[20:23], %[lb1] offset:28672\n\t" \
"v_accvgpr_read_b32 v150, a89\n\t" \
"v_accvgpr_read_b32 v151, a91\n\t" \
"v_mfma_f32_16x16x32_bf16 v[248:251], v[24:27], a[72:75], v[248:251]\n\t" \
"v_accvgpr_mov_b32 a89, a92\n\t" \
"v_accvgpr_mov_b32 a91, a94\n\t" \
"v_mfma_f32_16x16x32_bf16 v[252:255], v[24:27], a[76:79], v[252:255]\n\t" \
"ds_read_b128 v[24:27], %[lb1] offset:29696\n\t" \
"v_accvgpr_write_b32 a92, v150\n\t" \
"v_accvgpr_write_b32 a94, v151\n\t" \
"s_waitcnt lgkmcnt(4)\n\t" \
"v_mfma_f32_16x16x32_bf16 v[192:195], v[28:31], a[80:83], v[192:195]\n\t" \
"v_accvgpr_read_b32 v152, a97\n\t" \
"v_accvgpr_read_b32 v153, a99\n\t" \
"v_mfma_f32_16x16x32_bf16 v[196:199], v[28:31], a[84:87], v[196:199]\n\t" \
"ds_read_b128 v[28:31], %[lb1] offset:30720\n\t" \
"v_accvgpr_mov_b32 a97, a100\n\t" \
"v_accvgpr_mov_b32 a99, a102\n\t" \
"v_mfma_f32_16x16x32_bf16 v[200:203], v[32:35], a[80:83], v[200:203]\n\t" \
"v_accvgpr_write_b32 a100, v152\n\t" \
"v_accvgpr_write_b32 a102, v153\n\t" \
"v_mfma_f32_16x16x32_bf16 v[204:207], v[32:35], a[84:87], v[204:207]\n\t" \
"ds_read_b128 v[32:35], %[lb1] offset:31744\n\t" \
"v_accvgpr_read_b32 v154, a105\n\t" \
"v_accvgpr_read_b32 v155, a107\n\t" \
"s_waitcnt lgkmcnt(4)\n\t" \
"v_mfma_f32_16x16x32_bf16 v[208:211], v[36:39], a[80:83], v[208:211]\n\t" \
"v_accvgpr_mov_b32 a105, a108\n\t" \
"v_accvgpr_mov_b32 a107, a110\n\t" \
"v_mfma_f32_16x16x32_bf16 v[212:215], v[36:39], a[84:87], v[212:215]\n\t" \
"ds_read_b128 v[36:39], %[lb1] offset:32768\n\t" \
"v_accvgpr_write_b32 a108, v154\n\t" \
"v_accvgpr_write_b32 a110, v155\n\t" \
"v_mfma_f32_16x16x32_bf16 v[216:219], v[40:43], a[80:83], v[216:219]\n\t" \
"v_add_f32 v12, v12, v14\n\t" \
"v_add_f32 v13, v13, v15\n\t" \
"v_mfma_f32_16x16x32_bf16 v[220:223], v[40:43], a[84:87], v[220:223]\n\t" \
"ds_read_b128 v[40:43], %[lb1] offset:33792\n\t" \
"v_mov_b32 v14, v12\n\t" \
"v_mov_b32 v15, v13\n\t" \
"s_waitcnt lgkmcnt(4)\n\t" \
"v_mfma_f32_16x16x32_bf16 v[224:227], v[20:23], a[80:83], v[224:227]\n\t" \
"s_add_u32 s61, %[sW7], 143360\n\t" \
"s_add_u32 m0, %[w7], 0\n\t" \
"s_nop 0\n\t" \
"buffer_load_dwordx4 %[lb0], %[srd], s61 offen offset:0 lds\n\t" \
"v_mfma_f32_16x16x32_bf16 v[228:231], v[20:23], a[84:87], v[228:231]\n\t" \
"ds_read_b128 v[20:23], %[lb1] offset:34816\n\t" \
"v_permlane16_swap_b32 v12, v14\n\t" \
"v_permlane16_swap_b32 v13, v15\n\t" \
"v_mfma_f32_16x16x32_bf16 v[232:235], v[24:27], a[80:83], v[232:235]\n\t" \
"s_add_u32 s62, %[sW2], 0\n\t" \
"buffer_load_dwordx4 a[128:131], %[lb0], %[srd], s62 offen offset:0\n\t" \
"v_mfma_f32_16x16x32_bf16 v[236:239], v[24:27], a[84:87], v[236:239]\n\t" \
"ds_read_b128 v[24:27], %[lb1] offset:35840\n\t" \
"v_add_f32 v12, v12, v14\n\t" \
"v_add_f32 v13, v13, v15\n\t" \
"v_mov_b32 v14, v12\n\t" \
"s_waitcnt lgkmcnt(4)\n\t" \
"v_mfma_f32_16x16x32_bf16 v[240:243], v[28:31], a[80:83], v[240:243]\n\t" \
"buffer_load_dwordx4 %[lb0], %[srd], s61 offen offset:1024 lds\n\t" \
"v_mfma_f32_16x16x32_bf16 v[244:247], v[28:31], a[84:87], v[244:247]\n\t" \
"ds_read_b128 v[28:31], %[lb1] offset:36864\n\t" \
"v_mov_b32 v15, v13\n\t" \
"v_permlane32_swap_b32 v12, v14\n\t" \
"s_nop 0\n\t" \
"v_permlane32_swap_b32 v13, v15\n\t" \
"v_mfma_f32_16x16x32_bf16 v[248:251], v[32:35], a[80:83], v[248:251]\n\t" \
"v_add_f32 v12, v12, v14\n\t" \
"v_add_f32 v13, v13, v15\n\t" \
"v_mfma_f32_16x16x32_bf16 v[252:255], v[32:35], a[84:87], v[252:255]\n\t" \
"ds_read_b128 v[32:35], %[lb1] offset:37888\n\t" \
"v_mul_f32 v12, 0x3d800000, v12\n\t" \
"v_mul_f32 v13, 0x3d800000, v13\n\t" \
"s_waitcnt lgkmcnt(4)\n\t" \
"v_mfma_f32_16x16x32_bf16 v[192:195], v[36:39], a[88:91], v[192:195]\n\t" \
"buffer_load_dwordx4 a[132:135], %[lb0], %[srd], s62 offen offset:1024\n\t" \
"v_mfma_f32_16x16x32_bf16 v[196:199], v[36:39], a[92:95], v[196:199]\n\t" \
"ds_read_b128 v[36:39], %[lb1] offset:38912\n\t" \
"v_sub_f32 v128, v128, v12\n\t" \
"v_sub_f32 v130, v130, v13\n\t" \
"v_sub_f32 v132, v132, v12\n\t" \
"v_mfma_f32_16x16x32_bf16 v[200:203], v[40:43], a[88:91], v[200:203]\n\t" \
"buffer_load_dwordx4 %[lb0], %[srd], s61 offen offset:2048 lds\n\t" \
"v_mfma_f32_16x16x32_bf16 v[204:207], v[40:43], a[92:95], v[204:207]\n\t" \
"ds_read_b128 v[40:43], %[lb1] offset:39936\n\t" \
"v_sub_f32 v134, v134, v13\n\t" \
"v_sub_f32 v136, v136, v12\n\t" \
"v_sub_f32 v138, v138, v13\n\t" \
"s_waitcnt lgkmcnt(4)\n\t" \
"v_mfma_f32_16x16x32_bf16 v[208:211], v[20:23], a[88:91], v[208:211]\n\t" \
"buffer_load_dwordx4 a[136:139], %[lb0], %[srd], s62 offen offset:2048\n\t" \
"v_mfma_f32_16x16x32_bf16 v[212:215], v[20:23], a[92:95], v[212:215]\n\t" \
"ds_read_b128 v[20:23], %[lb1] offset:40960\n\t" \
"v_sub_f32 v140, v140, v12\n\t" \
"v_sub_f32 v142, v142, v13\n\t" \
"v_mul_f32 v128, v128, v128\n\t" \
"v_mfma_f32_16x16x32_bf16 v[216:219], v[24:27], a[88:91], v[216:219]\n\t" \
"v_mul_f32 v130, v130, v130\n\t" \
"v_mul_f32 v132, v132, v132\n\t" \
"v_mfma_f32_16x16x32_bf16 v[220:223], v[24:27], a[92:95], v[220:223]\n\t" \
"ds_read_b128 v[24:27], %[lb1] offset:41984\n\t" \
"v_mul_f32 v134, v134, v134\n\t" \
"v_mul_f32 v136, v136, v136\n\t" \
"s_waitcnt lgkmcnt(4)\n\t" \
"v_mfma_f32_16x16x32_bf16 v[224:227], v[28:31], a[88:91], v[224:227]\n\t" \
"buffer_load_dwordx4 %[lb0], %[srd], s61 offen offset:3072 lds\n\t" \
"v_mfma_f32_16x16x32_bf16 v[228:231], v[28:31], a[92:95], v[228:231]\n\t" \
"ds_read_b128 v[28:31], %[lb1] offset:43008\n\t" \
"v_mul_f32 v138, v138, v138\n\t" \
"v_mul_f32 v140, v140, v140\n\t" \
"v_mul_f32 v142, v142, v142\n\t" \
"v_mfma_f32_16x16x32_bf16 v[232:235], v[32:35], a[88:91], v[232:235]\n\t" \
"buffer_load_dwordx4 a[140:143], %[lb0], %[srd], s62 offen offset:3072\n\t" \
"v_mfma_f32_16x16x32_bf16 v[236:239], v[32:35], a[92:95], v[236:239]\n\t" \
"ds_read_b128 v[32:35], %[lb1] offset:44032\n\t" \
"v_fmac_f32 v129, 0x42000000, v128\n\t" \
"v_fmac_f32 v131, 0x42000000, v130\n\t" \
"v_fmac_f32 v133, 0x42000000, v132\n\t" \
"s_waitcnt lgkmcnt(4)\n\t" \
"v_mfma_f32_16x16x32_bf16 v[240:243], v[36:39], a[88:91], v[240:243]\n\t" \
"s_add_u32 s61, %[sW7], 147456\n\t" \
"s_add_u32 m0, %[w7], 4096\n\t" \
"s_nop 0\n\t" \
"buffer_load_dwordx4 %[lb0], %[srd], s61 offen offset:0 lds\n\t" \
"v_mfma_f32_16x16x32_bf16 v[244:247], v[36:39], a[92:95], v[244:247]\n\t" \
"ds_read_b128 v[36:39], %[lb1] offset:45056\n\t" \
"v_fmac_f32 v135, 0x42000000, v134\n\t" \
"v_fmac_f32 v137, 0x42000000, v136\n\t" \
"v_mfma_f32_16x16x32_bf16 v[248:251], v[40:43], a[88:91], v[248:251]\n\t" \
"v_fmac_f32 v139, 0x42000000, v138\n\t" \
"v_fmac_f32 v141, 0x42000000, v140\n\t" \
"v_mfma_f32_16x16x32_bf16 v[252:255], v[40:43], a[92:95], v[252:255]\n\t" \
"ds_read_b128 v[40:43], %[lb1] offset:46080\n\t" \
"v_fmac_f32 v143, 0x42000000, v142\n\t" \
"v_add_f32 v14, v129, v133\n\t" \
"s_waitcnt lgkmcnt(4)\n\t" \
"v_mfma_f32_16x16x32_bf16 v[192:195], v[20:23], a[96:99], v[192:195]\n\t" \
"s_add_u32 s62, %[sW2], 4096\n\t" \
"buffer_load_dwordx4 a[144:147], %[lb0], %[srd], s62 offen offset:0\n\t" \
"v_mfma_f32_16x16x32_bf16 v[196:199], v[20:23], a[100:103], v[196:199]\n\t" \
"ds_read_b128 v[20:23], %[lb1] offset:47104\n\t" \
"v_add_f32 v16, v137, v141\n\t" \
"v_add_f32 v15, v131, v135\n\t" \
"v_mfma_f32_16x16x32_bf16 v[200:203], v[24:27], a[96:99], v[200:203]\n\t" \
"buffer_load_dwordx4 %[lb0], %[srd], s61 offen offset:1024 lds\n\t" \
"v_mfma_f32_16x16x32_bf16 v[204:207], v[24:27], a[100:103], v[204:207]\n\t" \
"ds_read_b128 v[24:27], %[lb1] offset:48128\n\t" \
"v_add_f32 v17, v139, v143\n\t" \
"v_add_f32 v14, v14, v16\n\t" \
"v_add_f32 v15, v15, v17\n\t" \
"s_waitcnt lgkmcnt(4)\n\t" \
"v_mfma_f32_16x16x32_bf16 v[208:211], v[28:31], a[96:99], v[208:211]\n\t" \
"buffer_load_dwordx4 a[148:151], %[lb0], %[srd], s62 offen offset:1024\n\t" \
"v_mfma_f32_16x16x32_bf16 v[212:215], v[28:31], a[100:103], v[212:215]\n\t" \
"ds_read_b128 v[28:31], %[lb1] offset:49152\n\t" \
"v_mov_b32 v16, v14\n\t" \
"v_mov_b32 v17, v15\n\t" \
"s_nop 0\n\t" \
"v_permlane16_swap_b32 v14, v16\n\t" \
"v_mfma_f32_16x16x32_bf16 v[216:219], v[32:35], a[96:99], v[216:219]\n\t" \
"v_permlane16_swap_b32 v15, v17\n\t" \
"v_add_f32 v14, v14, v16\n\t" \
"v_mfma_f32_16x16x32_bf16 v[220:223], v[32:35], a[100:103], v[220:223]\n\t" \
"ds_read_b128 v[32:35], %[lb1] offset:50176\n\t" \
"v_add_f32 v15, v15, v17\n\t" \
"v_mov_b32 v16, v14\n\t" \
"s_waitcnt lgkmcnt(4)\n\t" \
"v_mfma_f32_16x16x32_bf16 v[224:227], v[36:39], a[96:99], v[224:227]\n\t" \
"buffer_load_dwordx4 %[lb0], %[srd], s61 offen offset:2048 lds\n\t" \
"v_mfma_f32_16x16x32_bf16 v[228:231], v[36:39], a[100:103], v[228:231]\n\t" \
"ds_read_b128 v[36:39], %[lb1] offset:51200\n\t" \
"v_mov_b32 v17, v15\n\t" \
"v_permlane32_swap_b32 v14, v16\n\t" \
"s_nop 0\n\t" \
"v_permlane32_swap_b32 v15, v17\n\t" \
"v_mfma_f32_16x16x32_bf16 v[232:235], v[40:43], a[96:99], v[232:235]\n\t" \
"buffer_load_dwordx4 a[152:155], %[lb0], %[srd], s62 offen offset:2048\n\t" \
"v_mfma_f32_16x16x32_bf16 v[236:239], v[40:43], a[100:103], v[236:239]\n\t" \
"ds_read_b128 v[40:43], %[lb1] offset:52224\n\t" \
"v_add_f32 v14, v14, v16\n\t" \
"v_add_f32 v15, v15, v17\n\t" \
"v_mov_b32 v16, 0x3727c5ac\n\t" \
"s_waitcnt lgkmcnt(4)\n\t" \
"v_mfma_f32_16x16x32_bf16 v[240:243], v[20:23], a[96:99], v[240:243]\n\t" \
"buffer_load_dwordx4 a[156:159], %[lb0], %[srd], s62 offen offset:3072\n\t" \
"v_mfma_f32_16x16x32_bf16 v[244:247], v[20:23], a[100:103], v[244:247]\n\t" \
"ds_read_b128 v[20:23], %[lb1] offset:53248\n\t" \
"v_fmamk_f32 v14, v14, 0x3b000000, v16\n\t" \
"v_fmamk_f32 v15, v15, 0x3b000000, v16\n\t" \
"v_rsq_f32 v16, v14\n\t" \
"v_mfma_f32_16x16x32_bf16 v[248:251], v[24:27], a[96:99], v[248:251]\n\t" \
"v_rsq_f32 v17, v15\n\t" \
"v_mfma_f32_16x16x32_bf16 v[252:255], v[24:27], a[100:103], v[252:255]\n\t" \
"ds_read_b128 v[24:27], %[lb1] offset:54272\n\t" \
"v_mul_f32 v136, v14, v16\n\t" \
"s_waitcnt lgkmcnt(4)\n\t" \
"v_mfma_f32_16x16x32_bf16 v[192:195], v[28:31], a[104:107], v[192:195]\n\t" \
"v_mul_f32 v137, v15, v17\n\t" \
"v_mul_f32 v60, 0xbfb8aa3b, v16\n\t" \
"v_mfma_f32_16x16x32_bf16 v[196:199], v[28:31], a[108:111], v[196:199]\n\t" \
"ds_read_b128 v[28:31], %[lb1] offset:55296\n\t" \
"v_mul_f32 v61, 0xbfb8aa3b, v16\n\t" \
"v_mul_f32 v62, 0xbfb8aa3b, v17\n\t" \
"v_mfma_f32_16x16x32_bf16 v[200:203], v[32:35], a[104:107], v[200:203]\n\t" \
"v_mul_f32 v63, 0xbfb8aa3b, v17\n\t" \
"v_mov_b32 v16, v15\n\t" \
"v_mfma_f32_16x16x32_bf16 v[204:207], v[32:35], a[108:111], v[204:207]\n\t" \
"ds_read_b128 v[32:35], %[lb1] offset:56320\n\t" \
"v_mov_b32 v17, v15\n\t" \
"v_mov_b32 v15, v14\n\t" \
"s_waitcnt lgkmcnt(4)\n\t" \
"v_mfma_f32_16x16x32_bf16 v[208:211], v[36:39], a[104:107], v[208:211]\n\t" \
"v_cvt_pk_bf16_f32 v129, -v12, v136\n\t" \
"v_cvt_pk_bf16_f32 v133, -v13, v137\n\t" \
"v_mfma_f32_16x16x32_bf16 v[212:215], v[36:39], a[108:111], v[212:215]\n\t" \
"s_waitcnt vmcnt(15)\n\t" \
"s_barrier\n\t" \
"ds_read_b128 v[36:39], %[lb2] offset:0\n\t" \
"v_accvgpr_read_b32 v156, a113\n\t" \
"v_accvgpr_read_b32 v157, a115\n\t" \
"v_mfma_f32_16x16x32_bf16 v[216:219], v[40:43], a[104:107], v[216:219]\n\t" \
"v_accvgpr_mov_b32 a113, a116\n\t" \
"v_accvgpr_mov_b32 a115, a118\n\t" \
"v_mfma_f32_16x16x32_bf16 v[220:223], v[40:43], a[108:111], v[220:223]\n\t" \
"ds_read_b128 v[40:43], %[lb2] offset:1024\n\t" \
"v_accvgpr_write_b32 a116, v156\n\t" \
"v_accvgpr_write_b32 a118, v157\n\t" \
"s_waitcnt lgkmcnt(4)\n\t" \
"v_mfma_f32_16x16x32_bf16 v[224:227], v[20:23], a[104:107], v[224:227]\n\t" \
"v_accvgpr_read_b32 v158, a121\n\t" \
"v_accvgpr_read_b32 v159, a123\n\t" \
"v_mfma_f32_16x16x32_bf16 v[228:231], v[20:23], a[108:111], v[228:231]\n\t" \
"ds_read_b128 v[20:23], %[lb2] offset:2048\n\t" \
"v_accvgpr_mov_b32 a121, a124\n\t" \
"v_accvgpr_mov_b32 a123, a126\n\t" \
"v_mfma_f32_16x16x32_bf16 v[232:235], v[24:27], a[104:107], v[232:235]\n\t" \
"v_accvgpr_write_b32 a124, v158\n\t" \
"v_accvgpr_write_b32 a126, v159\n\t" \
"v_mfma_f32_16x16x32_bf16 v[236:239], v[24:27], a[108:111], v[236:239]\n\t" \
"ds_read_b128 v[24:27], %[lb2] offset:3072\n\t" \
"v_lshlrev_b32 v138, 16, v129\n\t" \
"v_and_b32 v140, 0xffff0000, v129\n\t" \
"s_waitcnt lgkmcnt(4)\n\t" \
"v_mfma_f32_16x16x32_bf16 v[240:243], v[28:31], a[104:107], v[240:243]\n\t" \
"v_lshlrev_b32 v139, 16, v133\n\t" \
"v_and_b32 v141, 0xffff0000, v133\n\t" \
"v_mfma_f32_16x16x32_bf16 v[244:247], v[28:31], a[108:111], v[244:247]\n\t" \
"ds_read_b128 v[28:31], %[lb2] offset:4096\n\t" \
"v_sub_f32 v142, -v12, v138\n\t" \
"v_sub_f32 v143, -v13, v139\n\t" \
"v_mfma_f32_16x16x32_bf16 v[248:251], v[32:35], a[104:107], v[248:251]\n\t" \
"v_sub_f32 v136, v136, v140\n\t" \
"v_sub_f32 v137, v137, v141\n\t" \
"v_mfma_f32_16x16x32_bf16 v[252:255], v[32:35], a[108:111], v[252:255]\n\t" \
"ds_read_b128 v[32:35], %[lb2] offset:5120\n\t" \
"v_cvt_pk_bf16_f32 v128, v138, v142\n\t" \
"v_cvt_pk_bf16_f32 v130, v136, v140\n\t" \
"s_waitcnt lgkmcnt(4)\n\t" \
"v_mfma_f32_16x16x32_bf16 v[192:195], v[36:39], a[112:115], v[192:195]\n\t" \
"s_add_u32 s61, %[sW7], 172032\n\t" \
"s_add_u32 m0, %[w7], 28672\n\t" \
"s_nop 0\n\t" \
"buffer_load_dwordx4 %[lb0], %[srd], s61 offen offset:0 lds\n\t" \
"v_mfma_f32_16x16x32_bf16 v[196:199], v[36:39], a[116:119], v[196:199]\n\t" \
"ds_read_b128 v[36:39], %[lb2] offset:6144\n\t" \
"v_mov_b32 v131, 0\n\t" \
"v_cvt_pk_bf16_f32 v132, v139, v143\n\t" \
"v_mfma_f32_16x16x32_bf16 v[200:203], v[40:43], a[112:115], v[200:203]\n\t" \
"s_add_u32 s62, %[sW2], 8192\n\t" \
"buffer_load_dwordx4 a[160:163], %[lb0], %[srd], s62 offen offset:0\n\t" \
"v_mfma_f32_16x16x32_bf16 v[204:207], v[40:43], a[116:119], v[204:207]\n\t" \
"ds_read_b128 v[40:43], %[lb2] offset:7168\n\t" \
"v_cvt_pk_bf16_f32 v134, v137, v141\n\t" \
"v_mov_b32 v135, 0\n\t" \
"s_mov_b32 exec_lo, 0\n\t" \
"v_mov_b32 v128, 0\n\t" \
"v_mov_b32 v129, 0\n\t" \
"v_mov_b32 v130, 0\n\t" \
"v_mov_b32 v132, 0\n\t" \
"v_mov_b32 v133, 0\n\t" \
"v_mov_b32 v134, 0\n\t" \
"s_mov_b64 exec, -1\n\t" \
"s_waitcnt lgkmcnt(4)\n\t" \
"v_mfma_f32_16x16x32_bf16 v[208:211], v[20:23], a[112:115], v[208:211]\n\t" \
"v_mfma_f32_16x16x32_bf16 v[212:215], v[20:23], a[116:119], v[212:215]\n\t" \
"ds_read_b128 v[20:23], %[lb2] offset:8192\n\t" \
"v_mfma_f32_16x16x32_bf16 v[216:219], v[24:27], a[112:115], v[216:219]\n\t" \
"buffer_load_dwordx4 %[lb0], %[srd], s61 offen offset:1024 lds\n\t" \
"v_mfma_f32_16x16x32_bf16 v[220:223], v[24:27], a[116:119], v[220:223]\n\t" \
"ds_read_b128 v[24:27], %[lb2] offset:9216\n\t" \
"s_waitcnt lgkmcnt(4)\n\t" \
"v_mfma_f32_16x16x32_bf16 v[224:227], v[28:31], a[112:115], v[224:227]\n\t" \
"v_mfma_f32_16x16x32_bf16 v[228:231], v[28:31], a[116:119], v[228:231]\n\t" \
"ds_read_b128 v[28:31], %[lb2] offset:10240\n\t" \
"v_mfma_f32_16x16x32_bf16 v[232:235], v[32:35], a[112:115], v[232:235]\n\t" \
"buffer_load_dwordx4 a[164:167], %[lb0], %[srd], s62 offen offset:1024\n\t" \
"v_mfma_f32_16x16x32_bf16 v[236:239], v[32:35], a[116:119], v[236:239]\n\t" \
"ds_read_b128 v[32:35], %[lb2] offset:11264\n\t" \
"s_waitcnt lgkmcnt(4)\n\t" \
"v_mfma_f32_16x16x32_bf16 v[240:243], v[36:39], a[112:115], v[240:243]\n\t" \
"buffer_load_dwordx4 %[lb0], %[srd], s61 offen offset:2048 lds\n\t" \
"v_mfma_f32_16x16x32_bf16 v[244:247], v[36:39], a[116:119], v[244:247]\n\t" \
"ds_read_b128 v[36:39], %[lb2] offset:12288\n\t" \
"v_mfma_f32_16x16x32_bf16 v[248:251], v[40:43], a[112:115], v[248:251]\n\t" \
"v_mfma_f32_16x16x32_bf16 v[252:255], v[40:43], a[116:119], v[252:255]\n\t" \
"ds_read_b128 v[40:43], %[lb2] offset:13312\n\t" \
"s_waitcnt lgkmcnt(4)\n\t" \
"v_mfma_f32_16x16x32_bf16 v[192:195], v[20:23], a[120:123], v[192:195]\n\t" \
"buffer_load_dwordx4 a[168:171], %[lb0], %[srd], s62 offen offset:2048\n\t" \
"v_mfma_f32_16x16x32_bf16 v[196:199], v[20:23], a[124:127], v[196:199]\n\t" \
"ds_read_b128 v[20:23], %[lb2] offset:14336\n\t" \
"v_mfma_f32_16x16x32_bf16 v[200:203], v[24:27], a[120:123], v[200:203]\n\t" \
"v_mfma_f32_16x16x32_bf16 v[204:207], v[24:27], a[124:127], v[204:207]\n\t" \
"ds_read_b128 v[24:27], %[lb2] offset:15360\n\t" \
"s_waitcnt lgkmcnt(4)\n\t" \
"v_mfma_f32_16x16x32_bf16 v[208:211], v[28:31], a[120:123], v[208:211]\n\t" \
"buffer_load_dwordx4 %[lb0], %[srd], s61 offen offset:3072 lds\n\t" \
"v_mfma_f32_16x16x32_bf16 v[212:215], v[28:31], a[124:127], v[212:215]\n\t" \
"ds_read_b128 v[28:31], v19 offset:0\n\t" \
"v_mfma_f32_16x16x32_bf16 v[216:219], v[32:35], a[120:123], v[216:219]\n\t" \
"buffer_load_dwordx4 a[172:175], %[lb0], %[srd], s62 offen offset:3072\n\t" \
"v_mfma_f32_16x16x32_bf16 v[220:223], v[32:35], a[124:127], v[220:223]\n\t" \
"ds_read_b128 v[32:35], v19 offset:512\n\t" \
"s_waitcnt lgkmcnt(4)\n\t" \
"v_mfma_f32_16x16x32_bf16 v[224:227], v[36:39], a[120:123], v[224:227]\n\t" \
"v_mfma_f32_16x16x32_bf16 v[228:231], v[36:39], a[124:127], v[228:231]\n\t" \
"ds_read_b128 v[36:39], v19 offset:1024\n\t" \
"v_mfma_f32_16x16x32_bf16 v[232:235], v[40:43], a[120:123], v[232:235]\n\t" \
"s_add_u32 s61, %[sW7], 176128\n\t" \
"s_add_u32 m0, %[w7], 32768\n\t" \
"s_nop 0\n\t" \
"buffer_load_dwordx4 %[lb0], %[srd], s61 offen offset:0 lds\n\t" \
"v_mfma_f32_16x16x32_bf16 v[236:239], v[40:43], a[124:127], v[236:239]\n\t" \
"ds_read_b128 v[40:43], v19 offset:1536\n\t" \
"s_waitcnt lgkmcnt(4)\n\t" \
"v_mfma_f32_16x16x32_bf16 v[240:243], v[20:23], a[120:123], v[240:243]\n\t" \
"v_mfma_f32_16x16x32_bf16 v[244:247], v[20:23], a[124:127], v[244:247]\n\t" \
"ds_read_b128 v[20:23], v19 offset:2048\n\t" \
"v_mfma_f32_16x16x32_bf16 v[248:251], v[24:27], a[120:123], v[248:251]\n\t" \
"s_add_u32 s62, %[sW2], 12288\n\t" \
"buffer_load_dwordx4 a[176:179], %[lb0], %[srd], s62 offen offset:0\n\t" \
"v_mfma_f32_16x16x32_bf16 v[252:255], v[24:27], a[124:127], v[252:255]\n\t" \
"ds_read_b128 v[24:27], v19 offset:2560\n\t" \
"s_waitcnt lgkmcnt(4)\n\t" \
"v_mfma_f32_16x16x32_bf16 v[192:195], v[28:31], v[128:131], v[192:195]\n\t" \
"buffer_load_dwordx4 %[lb0], %[srd], s61 offen offset:1024 lds\n\t" \
"v_mfma_f32_16x16x32_bf16 v[196:199], v[28:31], v[132:135], v[196:199]\n\t" \
"ds_read_b128 v[28:31], v19 offset:3072\n\t" \
"v_mfma_f32_16x16x32_bf16 v[200:203], v[32:35], v[128:131], v[200:203]\n\t" \
"v_mfma_f32_16x16x32_bf16 v[204:207], v[32:35], v[132:135], v[204:207]\n\t" \
"ds_read_b128 v[32:35], v19 offset:3584\n\t" \
"s_waitcnt lgkmcnt(4)\n\t" \
"v_mfma_f32_16x16x32_bf16 v[208:211], v[36:39], v[128:131], v[208:211]\n\t" \
"buffer_load_dwordx4 a[180:183], %[lb0], %[srd], s62 offen offset:1024\n\t" \
"v_mfma_f32_16x16x32_bf16 v[212:215], v[36:39], v[132:135], v[212:215]\n\t" \
"ds_read_b128 v[36:39], v19 offset:4096\n\t" \
"v_mfma_f32_16x16x32_bf16 v[216:219], v[40:43], v[128:131], v[216:219]\n\t" \
"v_mfma_f32_16x16x32_bf16 v[220:223], v[40:43], v[132:135], v[220:223]\n\t" \
"ds_read_b128 v[40:43], v19 offset:4608\n\t" \
"s_waitcnt lgkmcnt(4)\n\t" \
"v_mfma_f32_16x16x32_bf16 v[224:227], v[20:23], v[128:131], v[224:227]\n\t" \
"buffer_load_dwordx4 %[lb0], %[srd], s61 offen offset:2048 lds\n\t" \
"v_mfma_f32_16x16x32_bf16 v[228:231], v[20:23], v[132:135], v[228:231]\n\t" \
"ds_read_b128 v[20:23], v19 offset:5120\n\t" \
"v_mfma_f32_16x16x32_bf16 v[232:235], v[24:27], v[128:131], v[232:235]\n\t" \
"buffer_load_dwordx4 a[184:187], %[lb0], %[srd], s62 offen offset:2048\n\t" \
"v_mfma_f32_16x16x32_bf16 v[236:239], v[24:27], v[132:135], v[236:239]\n\t" \
"ds_read_b128 v[24:27], v19 offset:5632\n\t" \
"v_mul_f32 v44, v192, v60\n\t" \
"v_mul_f32 v45, v193, v60\n\t" \
"v_mul_f32 v46, v194, v60\n\t" \
"v_mul_f32 v47, v195, v60\n\t" \
"v_exp_f32 v44, v44\n\t" \
"s_waitcnt lgkmcnt(4)\n\t" \
"v_mfma_f32_16x16x32_bf16 v[240:243], v[28:31], v[128:131], v[240:243]\n\t" \
"v_mul_f32 v192, v192, v200\n\t" \
"v_mul_f32 v52, v196, v62\n\t" \
"v_mfma_f32_16x16x32_bf16 v[244:247], v[28:31], v[132:135], v[244:247]\n\t" \
"ds_read_b128 v[28:31], %[lb2] offset:16384\n\t" \
"v_exp_f32 v45, v45\n\t" \
"v_mfma_f32_16x16x32_bf16 v[248:251], v[32:35], v[128:131], v[248:251]\n\t" \
"buffer_load_dwordx4 a[188:191], %[lb0], %[srd], s62 offen offset:3072\n\t" \
"v_mfma_f32_16x16x32_bf16 v[252:255], v[32:35], v[132:135], v[252:255]\n\t" \
"ds_read_b128 v[32:35], %[lb2] offset:17408\n\t" \
"v_mul_f32 v53, v197, v62\n\t" \
"v_mul_f32 v193, v193, v201\n\t" \
"v_mul_f32 v54, v198, v62\n\t" \
"v_exp_f32 v46, v46\n\t" \
"s_waitcnt lgkmcnt(4)\n\t" \
"v_mfma_f32_16x16x32_bf16 v[160:163], v[36:39], v[128:131], 0\n\t" \
"v_mul_f32 v55, v199, v62\n\t" \
"v_mul_f32 v194, v194, v202\n\t" \
"v_mfma_f32_16x16x32_bf16 v[164:167], v[36:39], v[132:135], 0\n\t" \
"ds_read_b128 v[36:39], %[lb2] offset:18432\n\t" \
"v_exp_f32 v52, v52\n\t" \
"v_mfma_f32_16x16x32_bf16 v[168:171], v[40:43], v[128:131], 0\n\t" \
"s_add_u32 s62, %[sW2], 16384\n\t" \
"buffer_load_dwordx4 a[192:195], %[lb0], %[srd], s62 offen offset:0\n\t" \
"v_mfma_f32_16x16x32_bf16 v[172:175], v[40:43], v[132:135], 0\n\t" \
"ds_read_b128 v[40:43], %[lb2] offset:19456\n\t" \
"v_exp_f32 v47, v47\n\t" \
"v_mul_f32 v196, v196, v204\n\t" \
"v_mul_f32 v195, v195, v203\n\t" \
"s_waitcnt lgkmcnt(4)\n\t" \
"v_mfma_f32_16x16x32_bf16 v[176:179], v[20:23], v[128:131], 0\n\t" \
"buffer_load_dwordx4 a[196:199], %[lb0], %[srd], s62 offen offset:1024\n\t" \
"v_mfma_f32_16x16x32_bf16 v[180:183], v[20:23], v[132:135], 0\n\t" \
"ds_read_b128 v[20:23], %[lb2] offset:20480\n\t" \
"v_exp_f32 v53, v53\n\t" \
"v_fma_f32 v44, v44, v14, v14\n\t" \
"v_mul_f32 v197, v197, v205\n\t" \
"v_mfma_f32_16x16x32_bf16 v[184:187], v[24:27], v[128:131], 0\n\t" \
"v_fma_f32 v45, v45, v14, v14\n\t" \
"v_exp_f32 v54, v54\n\t" \
"v_mfma_f32_16x16x32_bf16 v[188:191], v[24:27], v[132:135], 0\n\t" \
"ds_read_b128 v[24:27], %[lb2] offset:21504\n\t" \
"v_fma_f32 v46, v46, v14, v14\n\t" \
"v_mul_f32 v198, v198, v206\n\t" \
"s_waitcnt lgkmcnt(4)\n\t" \
"v_mfma_f32_16x16x32_bf16 v[160:163], v[28:31], a[0:3], v[160:163]\n\t" \
"buffer_load_dwordx4 a[200:203], %[lb0], %[srd], s62 offen offset:2048\n\t" \
"v_mfma_f32_16x16x32_bf16 v[164:167], v[28:31], a[4:7], v[164:167]\n\t" \
"ds_read_b128 v[28:31], %[lb2] offset:22528\n\t" \
"v_fma_f32 v47, v47, v14, v14\n\t" \
"v_exp_f32 v55, v55\n\t" \
"v_rcp_f32 v44, v44\n\t" \
"v_mfma_f32_16x16x32_bf16 v[168:171], v[32:35], a[0:3], v[168:171]\n\t" \
"v_mul_f32 v199, v199, v207\n\t" \
"v_rcp_f32 v45, v45\n\t" \
"v_mfma_f32_16x16x32_bf16 v[172:175], v[32:35], a[4:7], v[172:175]\n\t" \
"ds_read_b128 v[32:35], %[lb2] offset:23552\n\t" \
"v_fma_f32 v52, v52, v16, v16\n\t" \
"s_waitcnt lgkmcnt(4)\n\t" \
"v_mfma_f32_16x16x32_bf16 v[176:179], v[36:39], a[0:3], v[176:179]\n\t" \
"buffer_load_dwordx4 a[204:207], %[lb0], %[srd], s62 offen offset:3072\n\t" \
"v_mfma_f32_16x16x32_bf16 v[180:183], v[36:39], a[4:7], v[180:183]\n\t" \
"ds_read_b128 v[36:39], %[lb2] offset:24576\n\t" \
"v_rcp_f32 v46, v46\n\t" \
"v_fma_f32 v53, v53, v16, v16\n\t" \
"v_rcp_f32 v47, v47\n\t" \
"v_mfma_f32_16x16x32_bf16 v[184:187], v[40:43], a[0:3], v[184:187]\n\t" \
"v_fma_f32 v54, v54, v16, v16\n\t" \
"v_mul_f32 v192, v192, v44\n\t" \
"v_mfma_f32_16x16x32_bf16 v[188:191], v[40:43], a[4:7], v[188:191]\n\t" \
"ds_read_b128 v[40:43], %[lb2] offset:25600\n\t" \
"v_fma_f32 v55, v55, v16, v16\n\t" \
"v_mul_f32 v193, v193, v45\n\t" \
"s_waitcnt lgkmcnt(4)\n\t" \
"v_mfma_f32_16x16x32_bf16 v[160:163], v[20:23], a[8:11], v[160:163]\n\t" \
"v_rcp_f32 v52, v52\n\t" \
"v_mfma_f32_16x16x32_bf16 v[164:167], v[20:23], a[12:15], v[164:167]\n\t" \
"ds_read_b128 v[20:23], %[lb2] offset:26624\n\t" \
"v_mul_f32 v194, v194, v46\n\t" \
"v_rcp_f32 v53, v53\n\t" \
"v_mfma_f32_16x16x32_bf16 v[168:171], v[24:27], a[8:11], v[168:171]\n\t" \
"v_mul_f32 v195, v195, v47\n\t" \
"v_rcp_f32 v54, v54\n\t" \
"v_mfma_f32_16x16x32_bf16 v[172:175], v[24:27], a[12:15], v[172:175]\n\t" \
"ds_read_b128 v[24:27], %[lb2] offset:27648\n\t" \
"v_cvt_pk_bf16_f32 v200, v192, v193\n\t" \
"v_rcp_f32 v55, v55\n\t" \
"s_waitcnt lgkmcnt(4)\n\t" \
"v_mfma_f32_16x16x32_bf16 v[176:179], v[28:31], a[8:11], v[176:179]\n\t" \
"v_cvt_pk_bf16_f32 v201, v194, v195\n\t" \
"v_mul_f32 v196, v196, v52\n\t" \
"v_mfma_f32_16x16x32_bf16 v[180:183], v[28:31], a[12:15], v[180:183]\n\t" \
"s_waitcnt vmcnt(21)\n\t" \
"s_barrier\n\t" \
"ds_read_b128 v[28:31], %[lb0] offset:0\n\t" \
"v_mul_f32 v44, v208, v60\n\t" \
"v_mul_f32 v197, v197, v53\n\t" \
"v_mfma_f32_16x16x32_bf16 v[184:187], v[32:35], a[8:11], v[184:187]\n\t" \
"v_mul_f32 v45, v209, v60\n\t" \
"v_mul_f32 v198, v198, v54\n\t" \
"v_mfma_f32_16x16x32_bf16 v[188:191], v[32:35], a[12:15], v[188:191]\n\t" \
"ds_read_b128 v[32:35], %[lb0] offset:1024\n\t" \
"v_mul_f32 v46, v210, v60\n\t" \
"v_mul_f32 v199, v199, v55\n\t" \
"v_mul_f32 v47, v211, v60\n\t" \
"s_waitcnt lgkmcnt(4)\n\t" \
"v_mfma_f32_16x16x32_bf16 v[160:163], v[36:39], a[16:19], v[160:163]\n\t" \
"v_cvt_pk_bf16_f32 v204, v196, v197\n\t" \
"v_exp_f32 v44, v44\n\t" \
"v_mfma_f32_16x16x32_bf16 v[164:167], v[36:39], a[20:23], v[164:167]\n\t" \
"ds_read_b128 v[36:39], %[lb0] offset:2048\n\t" \
"v_cvt_pk_bf16_f32 v205, v198, v199\n\t" \
"v_mul_f32 v208, v208, v216\n\t" \
"v_mfma_f32_16x16x32_bf16 v[168:171], v[40:43], a[16:19], v[168:171]\n\t" \
"v_mul_f32 v52, v212, v62\n\t" \
"v_exp_f32 v45, v45\n\t" \
"v_mfma_f32_16x16x32_bf16 v[172:175], v[40:43], a[20:23], v[172:175]\n\t" \
"ds_read_b128 v[40:43], %[lb0] offset:3072\n\t" \
"v_mul_f32 v53, v213, v62\n\t" \
"v_mul_f32 v209, v209, v217\n\t" \
"s_waitcnt lgkmcnt(4)\n\t" \
"v_mfma_f32_16x16x32_bf16 v[176:179], v[20:23], a[16:19], v[176:179]\n\t" \
"v_mul_f32 v54, v214, v62\n\t" \
"v_exp_f32 v46, v46\n\t" \
"v_mfma_f32_16x16x32_bf16 v[180:183], v[20:23], a[20:23], v[180:183]\n\t" \
"ds_read_b128 v[20:23], %[lb0] offset:4096\n\t" \
"v_mul_f32 v55, v215, v62\n\t" \
"v_mul_f32 v210, v210, v218\n\t" \
"v_mfma_f32_16x16x32_bf16 v[184:187], v[24:27], a[16:19], v[184:187]\n\t" \
"v_exp_f32 v52, v52\n\t" \
"v_mfma_f32_16x16x32_bf16 v[188:191], v[24:27], a[20:23], v[188:191]\n\t" \
"ds_read_b128 v[24:27], %[lb0] offset:5120\n\t" \
"v_exp_f32 v47, v47\n\t" \
"v_mul_f32 v212, v212, v220\n\t" \
"s_waitcnt lgkmcnt(4)\n\t" \
"v_mfma_f32_16x16x32_bf16 v[160:163], v[28:31], a[24:27], v[160:163]\n\t" \
"s_add_u32 s61, %[sW7], 200704\n\t" \
"s_add_u32 m0, %[w7], 57344\n\t" \
"s_nop 0\n\t" \
"buffer_load_dwordx4 %[lb0], %[srd], s61 offen offset:0 lds\n\t" \
"v_mfma_f32_16x16x32_bf16 v[164:167], v[28:31], a[28:31], v[164:167]\n\t" \
"ds_read_b128 v[28:31], %[lb0] offset:6144\n\t" \
"v_mul_f32 v211, v211, v219\n\t" \
"v_exp_f32 v53, v53\n\t" \
"v_mfma_f32_16x16x32_bf16 v[168:171], v[32:35], a[24:27], v[168:171]\n\t" \
"s_add_u32 s62, %[sW2], 20480\n\t" \
"buffer_load_dwordx4 a[208:211], %[lb0], %[srd], s62 offen offset:0\n\t" \
"v_mfma_f32_16x16x32_bf16 v[172:175], v[32:35], a[28:31], v[172:175]\n\t" \
"ds_read_b128 v[32:35], %[lb0] offset:7168\n\t" \
"v_fma_f32 v44, v44, v14, v14\n\t" \
"v_mul_f32 v213, v213, v221\n\t" \
"v_fma_f32 v45, v45, v14, v14\n\t" \
"s_waitcnt lgkmcnt(4)\n\t" \
"v_mfma_f32_16x16x32_bf16 v[176:179], v[36:39], a[24:27], v[176:179]\n\t" \
"buffer_load_dwordx4 %[lb0], %[srd], s61 offen offset:1024 lds\n\t" \
"v_mfma_f32_16x16x32_bf16 v[180:183], v[36:39], a[28:31], v[180:183]\n\t" \
"ds_read_b128 v[36:39], %[lb0] offset:8192\n\t" \
"v_exp_f32 v54, v54\n\t" \
"v_fma_f32 v46, v46, v14, v14\n\t" \
"v_mul_f32 v214, v214, v222\n\t" \
"v_mfma_f32_16x16x32_bf16 v[184:187], v[40:43], a[24:27], v[184:187]\n\t" \
"buffer_load_dwordx4 a[212:215], %[lb0], %[srd], s62 offen offset:1024\n\t" \
"v_mfma_f32_16x16x32_bf16 v[188:191], v[40:43], a[28:31], v[188:191]\n\t" \
"ds_read_b128 v[40:43], %[lb0] offset:9216\n\t" \
"v_fma_f32 v47, v47, v14, v14\n\t" \
"v_exp_f32 v55, v55\n\t" \
"v_rcp_f32 v44, v44\n\t" \
"s_waitcnt lgkmcnt(4)\n\t" \
"v_mfma_f32_16x16x32_bf16 v[160:163], v[20:23], a[32:35], v[160:163]\n\t" \
"buffer_load_dwordx4 %[lb0], %[srd], s61 offen offset:2048 lds\n\t" \
"v_mfma_f32_16x16x32_bf16 v[164:167], v[20:23], a[36:39], v[164:167]\n\t" \
"ds_read_b128 v[20:23], %[lb0] offset:10240\n\t" \
"v_mul_f32 v215, v215, v223\n\t" \
"v_rcp_f32 v45, v45\n\t" \
"v_mfma_f32_16x16x32_bf16 v[168:171], v[24:27], a[32:35], v[168:171]\n\t" \
"buffer_load_dwordx4 a[216:219], %[lb0], %[srd], s62 offen offset:2048\n\t" \
"v_mfma_f32_16x16x32_bf16 v[172:175], v[24:27], a[36:39], v[172:175]\n\t" \
"ds_read_b128 v[24:27], %[lb0] offset:11264\n\t" \
"v_fma_f32 v52, v52, v16, v16\n\t" \
"v_rcp_f32 v46, v46\n\t" \
"v_fma_f32 v53, v53, v16, v16\n\t" \
"s_waitcnt lgkmcnt(4)\n\t" \
"v_mfma_f32_16x16x32_bf16 v[176:179], v[28:31], a[32:35], v[176:179]\n\t" \
"v_rcp_f32 v47, v47\n\t" \
"v_mfma_f32_16x16x32_bf16 v[180:183], v[28:31], a[36:39], v[180:183]\n\t" \
"ds_read_b128 v[28:31], %[lb0] offset:12288\n\t" \
"v_fma_f32 v54, v54, v16, v16\n\t" \
"v_mul_f32 v208, v208, v44\n\t" \
"v_fma_f32 v55, v55, v16, v16\n\t" \
"v_mfma_f32_16x16x32_bf16 v[184:187], v[32:35], a[32:35], v[184:187]\n\t" \
"buffer_load_dwordx4 %[lb0], %[srd], s61 offen offset:3072 lds\n\t" \
"v_mfma_f32_16x16x32_bf16 v[188:191], v[32:35], a[36:39], v[188:191]\n\t" \
"ds_read_b128 v[32:35], %[lb0] offset:13312\n\t" \
"v_mul_f32 v209, v209, v45\n\t" \
"v_rcp_f32 v52, v52\n\t" \
"v_mul_f32 v210, v210, v46\n\t" \
"s_waitcnt lgkmcnt(4)\n\t" \
"v_mfma_f32_16x16x32_bf16 v[160:163], v[36:39], a[40:43], v[160:163]\n\t" \
"buffer_load_dwordx4 a[220:223], %[lb0], %[srd], s62 offen offset:3072\n\t" \
"v_mfma_f32_16x16x32_bf16 v[164:167], v[36:39], a[44:47], v[164:167]\n\t" \
"ds_read_b128 v[36:39], %[lb0] offset:14336\n\t" \
"v_rcp_f32 v53, v53\n\t" \
"v_mul_f32 v211, v211, v47\n\t" \
"v_rcp_f32 v54, v54\n\t" \
"v_mfma_f32_16x16x32_bf16 v[168:171], v[40:43], a[40:43], v[168:171]\n\t" \
"s_add_u32 s61, %[sW7], 204800\n\t" \
"s_add_u32 m0, %[w7], 61440\n\t" \
"s_nop 0\n\t" \
"buffer_load_dwordx4 %[lb0], %[srd], s61 offen offset:0 lds\n\t" \
"v_mfma_f32_16x16x32_bf16 v[172:175], v[40:43], a[44:47], v[172:175]\n\t" \
"ds_read_b128 v[40:43], %[lb0] offset:15360\n\t" \
"v_cvt_pk_bf16_f32 v202, v208, v209\n\t" \
"v_rcp_f32 v55, v55\n\t" \
"s_waitcnt lgkmcnt(4)\n\t" \
"v_mfma_f32_16x16x32_bf16 v[176:179], v[20:23], a[40:43], v[176:179]\n\t" \
"s_add_u32 s62, %[sW2], 24576\n\t" \
"buffer_load_dwordx4 a[224:227], %[lb0], %[srd], s62 offen offset:0\n\t" \
"v_mfma_f32_16x16x32_bf16 v[180:183], v[20:23], a[44:47], v[180:183]\n\t" \
"ds_read_b128 v[20:23], %[lb0] offset:16384\n\t" \
"v_cvt_pk_bf16_f32 v203, v210, v211\n\t" \
"v_mul_f32 v212, v212, v52\n\t" \
"s_add_u32 s66, %[sH2s], 0\n\t" \
"buffer_store_dwordx4 v[200:203], %[lb0], %[srd], s66 offen sc1\n\t" \
"v_mfma_f32_16x16x32_bf16 v[184:187], v[24:27], a[40:43], v[184:187]\n\t" \
"buffer_load_dwordx4 %[lb0], %[srd], s61 offen offset:1024 lds\n\t" \
"v_mfma_f32_16x16x32_bf16 v[188:191], v[24:27], a[44:47], v[188:191]\n\t" \
"ds_read_b128 v[24:27], %[lb0] offset:17408\n\t" \
"v_mul_f32 v213, v213, v53\n\t" \
"v_mul_f32 v214, v214, v54\n\t" \
"v_mul_f32 v215, v215, v55\n\t" \
"s_waitcnt lgkmcnt(4)\n\t" \
"v_mfma_f32_16x16x32_bf16 v[160:163], v[28:31], a[48:51], v[160:163]\n\t" \
"buffer_load_dwordx4 a[228:231], %[lb0], %[srd], s62 offen offset:1024\n\t" \
"v_mfma_f32_16x16x32_bf16 v[164:167], v[28:31], a[52:55], v[164:167]\n\t" \
"ds_read_b128 v[28:31], %[lb0] offset:18432\n\t" \
"v_cvt_pk_bf16_f32 v206, v212, v213\n\t" \
"v_cvt_pk_bf16_f32 v207, v214, v215\n\t" \
"buffer_store_dwordx4 v[204:207], %[lb0], %[srd], s66 offen offset:1024 sc1\n\t" \
"v_mfma_f32_16x16x32_bf16 v[168:171], v[32:35], a[48:51], v[168:171]\n\t" \
"v_mul_f32 v44, v224, v60\n\t" \
"v_mul_f32 v45, v225, v60\n\t" \
"v_mfma_f32_16x16x32_bf16 v[172:175], v[32:35], a[52:55], v[172:175]\n\t" \
"ds_read_b128 v[32:35], %[lb0] offset:19456\n\t" \
"v_mul_f32 v46, v226, v60\n\t" \
"v_mul_f32 v47, v227, v60\n\t" \
"v_exp_f32 v44, v44\n\t" \
"s_waitcnt lgkmcnt(4)\n\t" \
"v_mfma_f32_16x16x32_bf16 v[176:179], v[36:39], a[48:51], v[176:179]\n\t" \
"buffer_load_dwordx4 %[lb0], %[srd], s61 offen offset:2048 lds\n\t" \
"v_mfma_f32_16x16x32_bf16 v[180:183], v[36:39], a[52:55], v[180:183]\n\t" \
"ds_read_b128 v[36:39], %[lb0] offset:20480\n\t" \
"v_mul_f32 v224, v224, v232\n\t" \
"v_mul_f32 v52, v228, v62\n\t" \
"v_exp_f32 v45, v45\n\t" \
"v_mfma_f32_16x16x32_bf16 v[184:187], v[40:43], a[48:51], v[184:187]\n\t" \
"buffer_load_dwordx4 a[232:235], %[lb0], %[srd], s62 offen offset:2048\n\t" \
"v_mfma_f32_16x16x32_bf16 v[188:191], v[40:43], a[52:55], v[188:191]\n\t" \
"ds_read_b128 v[40:43], %[lb0] offset:21504\n\t" \
"v_mul_f32 v53, v229, v62\n\t" \
"v_mul_f32 v225, v225, v233\n\t" \
"v_mul_f32 v54, v230, v62\n\t" \
"s_waitcnt lgkmcnt(4)\n\t" \
"v_mfma_f32_16x16x32_bf16 v[160:163], v[20:23], a[56:59], v[160:163]\n\t" \
"buffer_load_dwordx4 a[236:239], %[lb0], %[srd], s62 offen offset:3072\n\t" \
"v_mfma_f32_16x16x32_bf16 v[164:167], v[20:23], a[60:63], v[164:167]\n\t" \
"ds_read_b128 v[20:23], %[lb0] offset:22528\n\t" \
"v_exp_f32 v46, v46\n\t" \
"v_mul_f32 v55, v231, v62\n\t" \
"v_mul_f32 v226, v226, v234\n\t" \
"v_mfma_f32_16x16x32_bf16 v[168:171], v[24:27], a[56:59], v[168:171]\n\t" \
"s_add_u32 s62, %[sW2], 28672\n\t" \
"buffer_load_dwordx4 a[240:243], %[lb0], %[srd], s62 offen offset:0\n\t" \
"v_mfma_f32_16x16x32_bf16 v[172:175], v[24:27], a[60:63], v[172:175]\n\t" \
"ds_read_b128 v[24:27], %[lb0] offset:23552\n\t" \
"v_exp_f32 v52, v52\n\t" \
"v_exp_f32 v47, v47\n\t" \
"s_waitcnt lgkmcnt(4)\n\t" \
"v_mfma_f32_16x16x32_bf16 v[176:179], v[28:31], a[56:59], v[176:179]\n\t" \
"buffer_load_dwordx4 a[244:247], %[lb0], %[srd], s62 offen offset:1024\n\t" \
"v_mfma_f32_16x16x32_bf16 v[180:183], v[28:31], a[60:63], v[180:183]\n\t" \
"ds_read_b128 v[28:31], %[lb0] offset:24576\n\t" \
"v_mul_f32 v228, v228, v236\n\t" \
"v_mul_f32 v227, v227, v235\n\t" \
"v_exp_f32 v53, v53\n\t" \
"v_mfma_f32_16x16x32_bf16 v[184:187], v[32:35], a[56:59], v[184:187]\n\t" \
"v_fma_f32 v44, v44, v14, v14\n\t" \
"v_mul_f32 v229, v229, v237\n\t" \
"v_mfma_f32_16x16x32_bf16 v[188:191], v[32:35], a[60:63], v[188:191]\n\t" \
"ds_read_b128 v[32:35], %[lb0] offset:25600\n\t" \
"v_fma_f32 v45, v45, v14, v14\n\t" \
"v_exp_f32 v54, v54\n\t" \
"s_waitcnt lgkmcnt(4)\n\t" \
"v_mfma_f32_16x16x32_bf16 v[160:163], v[36:39], a[64:67], v[160:163]\n\t" \
"v_fma_f32 v46, v46, v14, v14\n\t" \
"v_mul_f32 v230, v230, v238\n\t" \
"v_mfma_f32_16x16x32_bf16 v[164:167], v[36:39], a[68:71], v[164:167]\n\t" \
"ds_read_b128 v[36:39], %[lb0] offset:26624\n\t" \
"v_fma_f32 v47, v47, v14, v14\n\t" \
"v_exp_f32 v55, v55\n\t" \
"v_mfma_f32_16x16x32_bf16 v[168:171], v[40:43], a[64:67], v[168:171]\n\t" \
"v_rcp_f32 v44, v44\n\t" \
"v_mfma_f32_16x16x32_bf16 v[172:175], v[40:43], a[68:71], v[172:175]\n\t" \
"ds_read_b128 v[40:43], %[lb0] offset:27648\n\t" \
"v_mul_f32 v231, v231, v239\n\t" \
"v_rcp_f32 v45, v45\n\t" \
"s_waitcnt lgkmcnt(4)\n\t" \
"v_mfma_f32_16x16x32_bf16 v[176:179], v[20:23], a[64:67], v[176:179]\n\t" \
"v_fma_f32 v52, v52, v16, v16\n\t" \
"v_rcp_f32 v46, v46\n\t" \
"v_mfma_f32_16x16x32_bf16 v[180:183], v[20:23], a[68:71], v[180:183]\n\t" \
"s_waitcnt vmcnt(25)\n\t" \
"s_barrier\n\t" \
"ds_read_b128 v[20:23], %[lb0] offset:28672\n\t" \
"v_fma_f32 v53, v53, v16, v16\n\t" \
"v_mfma_f32_16x16x32_bf16 v[184:187], v[24:27], a[64:67], v[184:187]\n\t" \
"buffer_load_dwordx4 a[248:251], %[lb0], %[srd], s62 offen offset:2048\n\t" \
"v_mfma_f32_16x16x32_bf16 v[188:191], v[24:27], a[68:71], v[188:191]\n\t" \
"ds_read_b128 v[24:27], %[lb0] offset:29696\n\t" \
"v_rcp_f32 v47, v47\n\t" \
"v_fma_f32 v54, v54, v16, v16\n\t" \
"v_mul_f32 v224, v224, v44\n\t" \
"s_waitcnt lgkmcnt(4)\n\t" \
"v_mfma_f32_16x16x32_bf16 v[160:163], v[28:31], a[72:75], v[160:163]\n\t" \
"v_fma_f32 v55, v55, v16, v16\n\t" \
"v_mul_f32 v225, v225, v45\n\t" \
"v_mfma_f32_16x16x32_bf16 v[164:167], v[28:31], a[76:79], v[164:167]\n\t" \
"ds_read_b128 v[28:31], %[lb0] offset:30720\n\t" \
"v_rcp_f32 v52, v52\n\t" \
"v_mul_f32 v226, v226, v46\n\t" \
"v_mfma_f32_16x16x32_bf16 v[168:171], v[32:35], a[72:75], v[168:171]\n\t" \
"buffer_load_dwordx4 a[252:255], %[lb0], %[srd], s62 offen offset:3072\n\t" \
"v_mfma_f32_16x16x32_bf16 v[172:175], v[32:35], a[76:79], v[172:175]\n\t" \
"ds_read_b128 v[32:35], %[lb0] offset:31744\n\t" \
"v_rcp_f32 v53, v53\n\t" \
"v_mul_f32 v227, v227, v47\n\t" \
"v_rcp_f32 v54, v54\n\t" \
"s_waitcnt lgkmcnt(4)\n\t" \
"v_mfma_f32_16x16x32_bf16 v[176:179], v[36:39], a[72:75], v[176:179]\n\t" \
"v_cvt_pk_bf16_f32 v232, v224, v225\n\t" \
"v_rcp_f32 v55, v55\n\t" \
"v_mfma_f32_16x16x32_bf16 v[180:183], v[36:39], a[76:79], v[180:183]\n\t" \
"ds_read_b128 v[36:39], %[lb0] offset:32768\n\t" \
"v_cvt_pk_bf16_f32 v233, v226, v227\n\t" \
"v_mfma_f32_16x16x32_bf16 v[184:187], v[40:43], a[72:75], v[184:187]\n\t" \
"v_mul_f32 v228, v228, v52\n\t" \
"v_mul_f32 v44, v240, v60\n\t" \
"v_mfma_f32_16x16x32_bf16 v[188:191], v[40:43], a[76:79], v[188:191]\n\t" \
"ds_read_b128 v[40:43], %[lb0] offset:33792\n\t" \
"v_mul_f32 v229, v229, v53\n\t" \
"v_mul_f32 v45, v241, v60\n\t" \
"v_mul_f32 v230, v230, v54\n\t" \
"s_waitcnt lgkmcnt(4)\n\t" \
"v_mfma_f32_16x16x32_bf16 v[160:163], v[20:23], a[80:83], v[160:163]\n\t" \
"s_add_u32 s62, %[sW2], 32768\n\t" \
"buffer_load_dwordx4 v[64:67], %[lb0], %[srd], s62 offen offset:0\n\t" \
"v_mfma_f32_16x16x32_bf16 v[164:167], v[20:23], a[84:87], v[164:167]\n\t" \
"ds_read_b128 v[20:23], %[lb0] offset:34816\n\t" \
"v_mul_f32 v46, v242, v60\n\t" \
"v_mul_f32 v231, v231, v55\n\t" \
"v_mul_f32 v47, v243, v60\n\t" \
"v_cvt_pk_bf16_f32 v236, v228, v229\n\t" \
"v_mfma_f32_16x16x32_bf16 v[168:171], v[24:27], a[80:83], v[168:171]\n\t" \
"v_exp_f32 v44, v44\n\t" \
"v_mfma_f32_16x16x32_bf16 v[172:175], v[24:27], a[84:87], v[172:175]\n\t" \
"ds_read_b128 v[24:27], %[lb0] offset:35840\n\t" \
"v_cvt_pk_bf16_f32 v237, v230, v231\n\t" \
"v_mul_f32 v240, v240, v248\n\t" \
"v_mul_f32 v52, v244, v62\n\t" \
"s_waitcnt lgkmcnt(4)\n\t" \
"v_mfma_f32_16x16x32_bf16 v[176:179], v[28:31], a[80:83], v[176:179]\n\t" \
"v_exp_f32 v45, v45\n\t" \
"v_mfma_f32_16x16x32_bf16 v[180:183], v[28:31], a[84:87], v[180:183]\n\t" \
"ds_read_b128 v[28:31], %[lb0] offset:36864\n\t" \
"v_mul_f32 v53, v245, v62\n\t" \
"v_mul_f32 v241, v241, v249\n\t" \
"v_mul_f32 v54, v246, v62\n\t" \
"v_mfma_f32_16x16x32_bf16 v[184:187], v[32:35], a[80:83], v[184:187]\n\t" \
"buffer_load_dwordx4 v[68:71], %[lb0], %[srd], s62 offen offset:1024\n\t" \
"v_mfma_f32_16x16x32_bf16 v[188:191], v[32:35], a[84:87], v[188:191]\n\t" \
"ds_read_b128 v[32:35], %[lb0] offset:37888\n\t" \
"v_exp_f32 v46, v46\n\t" \
"v_mul_f32 v55, v247, v62\n\t" \
"v_mul_f32 v242, v242, v250\n\t" \
"s_waitcnt lgkmcnt(4)\n\t" \
"v_mfma_f32_16x16x32_bf16 v[160:163], v[36:39], a[88:91], v[160:163]\n\t" \
"v_exp_f32 v52, v52\n\t" \
"v_mfma_f32_16x16x32_bf16 v[164:167], v[36:39], a[92:95], v[164:167]\n\t" \
"ds_read_b128 v[36:39], %[lb0] offset:38912\n\t" \
"v_exp_f32 v47, v47\n\t" \
"v_mul_f32 v244, v244, v252\n\t" \
"v_mfma_f32_16x16x32_bf16 v[168:171], v[40:43], a[88:91], v[168:171]\n\t" \
"v_mul_f32 v243, v243, v251\n\t" \
"v_exp_f32 v53, v53\n\t" \
"v_mfma_f32_16x16x32_bf16 v[172:175], v[40:43], a[92:95], v[172:175]\n\t" \
"ds_read_b128 v[40:43], %[lb0] offset:39936\n\t" \
"v_fma_f32 v44, v44, v14, v14\n\t" \
"v_mul_f32 v245, v245, v253\n\t" \
"s_waitcnt lgkmcnt(4)\n\t" \
"v_mfma_f32_16x16x32_bf16 v[176:179], v[20:23], a[88:91], v[176:179]\n\t" \
"buffer_load_dwordx4 v[72:75], %[lb0], %[srd], s62 offen offset:2048\n\t" \
"v_mfma_f32_16x16x32_bf16 v[180:183], v[20:23], a[92:95], v[180:183]\n\t" \
"ds_read_b128 v[20:23], %[lb0] offset:40960\n\t" \
"v_fma_f32 v45, v45, v14, v14\n\t" \
"v_exp_f32 v54, v54\n\t" \
"v_fma_f32 v46, v46, v14, v14\n\t" \
"v_mfma_f32_16x16x32_bf16 v[184:187], v[24:27], a[88:91], v[184:187]\n\t" \
"v_mul_f32 v246, v246, v254\n\t" \
"v_fma_f32 v47, v47, v14, v14\n\t" \
"v_mfma_f32_16x16x32_bf16 v[188:191], v[24:27], a[92:95], v[188:191]\n\t" \
"ds_read_b128 v[24:27], %[lb0] offset:41984\n\t" \
"v_exp_f32 v55, v55\n\t" \
"v_rcp_f32 v44, v44\n\t" \
"s_waitcnt lgkmcnt(4)\n\t" \
"v_mfma_f32_16x16x32_bf16 v[160:163], v[28:31], a[96:99], v[160:163]\n\t" \
"v_mul_f32 v247, v247, v255\n\t" \
"v_rcp_f32 v45, v45\n\t" \
"v_mfma_f32_16x16x32_bf16 v[164:167], v[28:31], a[100:103], v[164:167]\n\t" \
"ds_read_b128 v[28:31], %[lb0] offset:43008\n\t" \
"v_fma_f32 v52, v52, v16, v16\n\t" \
"v_mfma_f32_16x16x32_bf16 v[168:171], v[32:35], a[96:99], v[168:171]\n\t" \
"buffer_load_dwordx4 v[76:79], %[lb0], %[srd], s62 offen offset:3072\n\t" \
"v_mfma_f32_16x16x32_bf16 v[172:175], v[32:35], a[100:103], v[172:175]\n\t" \
"ds_read_b128 v[32:35], %[lb0] offset:44032\n\t" \
"v_rcp_f32 v46, v46\n\t" \
"v_fma_f32 v53, v53, v16, v16\n\t" \
"v_rcp_f32 v47, v47\n\t" \
"s_waitcnt lgkmcnt(4)\n\t" \
"v_mfma_f32_16x16x32_bf16 v[176:179], v[36:39], a[96:99], v[176:179]\n\t" \
"v_fma_f32 v54, v54, v16, v16\n\t" \
"v_mul_f32 v240, v240, v44\n\t" \
"v_mfma_f32_16x16x32_bf16 v[180:183], v[36:39], a[100:103], v[180:183]\n\t" \
"ds_read_b128 v[36:39], %[lb0] offset:45056\n\t" \
"v_fma_f32 v55, v55, v16, v16\n\t" \
"v_mul_f32 v241, v241, v45\n\t" \
"v_mfma_f32_16x16x32_bf16 v[184:187], v[40:43], a[96:99], v[184:187]\n\t" \
"v_rcp_f32 v52, v52\n\t" \
"v_mfma_f32_16x16x32_bf16 v[188:191], v[40:43], a[100:103], v[188:191]\n\t" \
"ds_read_b128 v[40:43], %[lb0] offset:46080\n\t" \
"v_mul_f32 v242, v242, v46\n\t" \
"v_rcp_f32 v53, v53\n\t" \
"s_waitcnt lgkmcnt(4)\n\t" \
"v_mfma_f32_16x16x32_bf16 v[160:163], v[20:23], a[104:107], v[160:163]\n\t" \
"s_add_u32 s62, %[sW2], 36864\n\t" \
"buffer_load_dwordx4 v[80:83], %[lb0], %[srd], s62 offen offset:0\n\t" \
"v_mfma_f32_16x16x32_bf16 v[164:167], v[20:23], a[108:111], v[164:167]\n\t" \
"ds_read_b128 v[20:23], %[lb0] offset:47104\n\t" \
"v_mul_f32 v243, v243, v47\n\t" \
"v_rcp_f32 v54, v54\n\t" \
"v_mfma_f32_16x16x32_bf16 v[168:171], v[24:27], a[104:107], v[168:171]\n\t" \
"v_cvt_pk_bf16_f32 v234, v240, v241\n\t" \
"v_rcp_f32 v55, v55\n\t" \
"v_mfma_f32_16x16x32_bf16 v[172:175], v[24:27], a[108:111], v[172:175]\n\t" \
"ds_read_b128 v[24:27], %[lb0] offset:48128\n\t" \
"v_cvt_pk_bf16_f32 v235, v242, v243\n\t" \
"v_mul_f32 v244, v244, v52\n\t" \
"s_waitcnt lgkmcnt(4)\n\t" \
"v_mfma_f32_16x16x32_bf16 v[176:179], v[28:31], a[104:107], v[176:179]\n\t" \
"s_add_u32 s66, %[sH2s], 524288\n\t" \
"buffer_store_dwordx4 v[232:235], %[lb0], %[srd], s66 offen sc1\n\t" \
"v_mfma_f32_16x16x32_bf16 v[180:183], v[28:31], a[108:111], v[180:183]\n\t" \
"ds_read_b128 v[28:31], %[lb0] offset:49152\n\t" \
"v_mul_f32 v245, v245, v53\n\t" \
"v_mul_f32 v246, v246, v54\n\t" \
"v_mul_f32 v247, v247, v55\n\t" \
"v_mfma_f32_16x16x32_bf16 v[184:187], v[32:35], a[104:107], v[184:187]\n\t" \
"buffer_load_dwordx4 v[84:87], %[lb0], %[srd], s62 offen offset:1024\n\t" \
"v_mfma_f32_16x16x32_bf16 v[188:191], v[32:35], a[108:111], v[188:191]\n\t" \
"ds_read_b128 v[32:35], %[lb0] offset:50176\n\t" \
"v_cvt_pk_bf16_f32 v238, v244, v245\n\t" \
"v_cvt_pk_bf16_f32 v239, v246, v247\n\t" \
"buffer_store_dwordx4 v[236:239], %[lb0], %[srd], s66 offen offset:1024 sc1\n\t" \
"s_waitcnt lgkmcnt(4)\n\t" \
"v_mfma_f32_16x16x32_bf16 v[160:163], v[36:39], a[112:115], v[160:163]\n\t" \
"v_mfma_f32_16x16x32_bf16 v[164:167], v[36:39], a[116:119], v[164:167]\n\t" \
"ds_read_b128 v[36:39], %[lb0] offset:51200\n\t" \
"v_mfma_f32_16x16x32_bf16 v[168:171], v[40:43], a[112:115], v[168:171]\n\t" \
"v_mfma_f32_16x16x32_bf16 v[172:175], v[40:43], a[116:119], v[172:175]\n\t" \
"ds_read_b128 v[40:43], %[lb0] offset:52224\n\t" \
"s_waitcnt lgkmcnt(4)\n\t" \
"v_mfma_f32_16x16x32_bf16 v[176:179], v[20:23], a[112:115], v[176:179]\n\t" \
"buffer_load_dwordx4 v[88:91], %[lb0], %[srd], s62 offen offset:2048\n\t" \
"v_mfma_f32_16x16x32_bf16 v[180:183], v[20:23], a[116:119], v[180:183]\n\t" \
"ds_read_b128 v[20:23], v19 offset:6144\n\t" \
"v_mfma_f32_16x16x32_bf16 v[184:187], v[24:27], a[112:115], v[184:187]\n\t" \
"v_mfma_f32_16x16x32_bf16 v[188:191], v[24:27], a[116:119], v[188:191]\n\t" \
"ds_read_b128 v[24:27], v19 offset:6656\n\t" \
"s_waitcnt lgkmcnt(4)\n\t" \
"v_mfma_f32_16x16x32_bf16 v[160:163], v[28:31], a[120:123], v[160:163]\n\t" \
"v_mfma_f32_16x16x32_bf16 v[164:167], v[28:31], a[124:127], v[164:167]\n\t" \
"ds_read_b128 v[28:31], %[lb0] offset:53248\n\t" \
"v_mfma_f32_16x16x32_bf16 v[168:171], v[32:35], a[120:123], v[168:171]\n\t" \
"buffer_load_dwordx4 v[92:95], %[lb0], %[srd], s62 offen offset:3072\n\t" \
"v_mfma_f32_16x16x32_bf16 v[172:175], v[32:35], a[124:127], v[172:175]\n\t" \
"ds_read_b128 v[32:35], %[lb0] offset:54272\n\t" \
"s_waitcnt lgkmcnt(4)\n\t" \
"v_mfma_f32_16x16x32_bf16 v[176:179], v[36:39], a[120:123], v[176:179]\n\t" \
"v_mfma_f32_16x16x32_bf16 v[180:183], v[36:39], a[124:127], v[180:183]\n\t" \
"ds_read_b128 v[36:39], %[lb0] offset:55296\n\t" \
"v_mfma_f32_16x16x32_bf16 v[184:187], v[40:43], a[120:123], v[184:187]\n\t" \
"v_mfma_f32_16x16x32_bf16 v[188:191], v[40:43], a[124:127], v[188:191]\n\t" \
"ds_read_b128 v[40:43], %[lb0] offset:56320\n\t" \
"s_waitcnt lgkmcnt(4)\n\t" \
"v_mfma_f32_16x16x32_bf16 v[144:147], v[20:23], v[128:131], 0\n\t" \
"v_mfma_f32_16x16x32_bf16 v[148:151], v[20:23], v[132:135], 0\n\t" \
"s_waitcnt vmcnt(16)\n\t" \
"s_barrier\n\t" \
"ds_read_b128 v[20:23], %[lb1] offset:0\n\t" \
"v_mfma_f32_16x16x32_bf16 v[152:155], v[24:27], v[128:131], 0\n\t" \
"v_mul_f32 v44, v160, v60\n\t" \
"v_mul_f32 v45, v161, v60\n\t" \
"v_mfma_f32_16x16x32_bf16 v[156:159], v[24:27], v[132:135], 0\n\t" \
"ds_read_b128 v[24:27], %[lb1] offset:1024\n\t" \
"v_mul_f32 v46, v162, v60\n\t" \
"v_mul_f32 v47, v163, v60\n\t" \
"v_exp_f32 v44, v44\n\t" \
"s_waitcnt lgkmcnt(4)\n\t" \
"v_mfma_f32_16x16x32_bf16 v[144:147], v[28:31], a[0:3], v[144:147]\n\t" \
"v_mul_f32 v160, v160, v168\n\t" \
"v_mul_f32 v52, v164, v62\n\t" \
"v_mfma_f32_16x16x32_bf16 v[148:151], v[28:31], a[4:7], v[148:151]\n\t" \
"ds_read_b128 v[28:31], %[lb1] offset:2048\n\t" \
"v_exp_f32 v45, v45\n\t" \
"v_mfma_f32_16x16x32_bf16 v[152:155], v[32:35], a[0:3], v[152:155]\n\t" \
"v_mul_f32 v53, v165, v62\n\t" \
"v_mul_f32 v161, v161, v169\n\t" \
"v_mfma_f32_16x16x32_bf16 v[156:159], v[32:35], a[4:7], v[156:159]\n\t" \
"ds_read_b128 v[32:35], %[lb1] offset:3072\n\t" \
"v_mul_f32 v54, v166, v62\n\t" \
"s_waitcnt lgkmcnt(4)\n\t" \
"v_mfma_f32_16x16x32_bf16 v[144:147], v[36:39], a[8:11], v[144:147]\n\t" \
"v_exp_f32 v46, v46\n\t" \
"v_mfma_f32_16x16x32_bf16 v[148:151], v[36:39], a[12:15], v[148:151]\n\t" \
"ds_read_b128 v[36:39], %[lb1] offset:4096\n\t" \
"v_mul_f32 v55, v167, v62\n\t" \
"v_mul_f32 v162, v162, v170\n\t" \
"v_mfma_f32_16x16x32_bf16 v[152:155], v[40:43], a[8:11], v[152:155]\n\t" \
"v_exp_f32 v52, v52\n\t" \
"v_mfma_f32_16x16x32_bf16 v[156:159], v[40:43], a[12:15], v[156:159]\n\t" \
"ds_read_b128 v[40:43], %[lb1] offset:5120\n\t" \
"v_exp_f32 v47, v47\n\t" \
"s_waitcnt lgkmcnt(4)\n\t" \
"v_mfma_f32_16x16x32_bf16 v[144:147], v[20:23], a[16:19], v[144:147]\n\t" \
"v_mul_f32 v164, v164, v172\n\t" \
"v_mul_f32 v163, v163, v171\n\t" \
"v_mfma_f32_16x16x32_bf16 v[148:151], v[20:23], a[20:23], v[148:151]\n\t" \
"ds_read_b128 v[20:23], %[lb1] offset:6144\n\t" \
"v_exp_f32 v53, v53\n\t" \
"v_mfma_f32_16x16x32_bf16 v[152:155], v[24:27], a[16:19], v[152:155]\n\t" \
"v_fma_f32 v44, v44, v14, v14\n\t" \
"v_mul_f32 v165, v165, v173\n\t" \
"v_mfma_f32_16x16x32_bf16 v[156:159], v[24:27], a[20:23], v[156:159]\n\t" \
"ds_read_b128 v[24:27], %[lb1] offset:7168\n\t" \
"v_fma_f32 v45, v45, v14, v14\n\t" \
"v_exp_f32 v54, v54\n\t" \
"s_waitcnt lgkmcnt(4)\n\t" \
"v_mfma_f32_16x16x32_bf16 v[144:147], v[28:31], a[24:27], v[144:147]\n\t" \
"v_fma_f32 v46, v46, v14, v14\n\t" \
"v_mul_f32 v166, v166, v174\n\t" \
"v_mfma_f32_16x16x32_bf16 v[148:151], v[28:31], a[28:31], v[148:151]\n\t" \
"ds_read_b128 v[28:31], %[lb1] offset:8192\n\t" \
"v_fma_f32 v47, v47, v14, v14\n\t" \
"v_mfma_f32_16x16x32_bf16 v[152:155], v[32:35], a[24:27], v[152:155]\n\t" \
"v_exp_f32 v55, v55\n\t" \
"v_mfma_f32_16x16x32_bf16 v[156:159], v[32:35], a[28:31], v[156:159]\n\t" \
"ds_read_b128 v[32:35], %[lb1] offset:9216\n\t" \
"v_rcp_f32 v44, v44\n\t" \
"s_waitcnt lgkmcnt(4)\n\t" \
"v_mfma_f32_16x16x32_bf16 v[144:147], v[36:39], a[32:35], v[144:147]\n\t" \
"v_mul_f32 v167, v167, v175\n\t" \
"v_rcp_f32 v45, v45\n\t" \
"v_mfma_f32_16x16x32_bf16 v[148:151], v[36:39], a[36:39], v[148:151]\n\t" \
"s_waitcnt vmcnt(0)\n\t" \
"s_barrier\n\t" \
"ds_read_b128 v[36:39], %[lb1] offset:10240\n\t" \
"s_mov_b64 exec, s[70:71]\n\t" \
"buffer_store_dword v18, off, %[srd], %[sFs] offset:16 sc1\n\t" \
"s_mov_b64 exec, -1\n\t" \
"v_mfma_f32_16x16x32_bf16 v[152:155], v[40:43], a[32:35], v[152:155]\n\t" \
"s_add_u32 s62, %[sW2], 40960\n\t" \
"buffer_load_dwordx4 v[96:99], %[lb0], %[srd], s62 offen offset:0\n\t" \
"v_mfma_f32_16x16x32_bf16 v[156:159], v[40:43], a[36:39], v[156:159]\n\t" \
"ds_read_b128 v[40:43], %[lb1] offset:11264\n\t" \
"s_waitcnt lgkmcnt(4)\n\t" \
"v_mfma_f32_16x16x32_bf16 v[144:147], v[20:23], a[40:43], v[144:147]\n\t" \
"buffer_load_dwordx4 v[100:103], %[lb0], %[srd], s62 offen offset:1024\n\t" \
"v_mfma_f32_16x16x32_bf16 v[148:151], v[20:23], a[44:47], v[148:151]\n\t" \
"ds_read_b128 v[20:23], %[lb1] offset:12288\n\t" \
"v_fma_f32 v52, v52, v16, v16\n\t" \
"v_rcp_f32 v46, v46\n\t" \
"v_mfma_f32_16x16x32_bf16 v[152:155], v[24:27], a[40:43], v[152:155]\n\t" \
"v_fma_f32 v53, v53, v16, v16\n\t" \
"v_rcp_f32 v47, v47\n\t" \
"v_mfma_f32_16x16x32_bf16 v[156:159], v[24:27], a[44:47], v[156:159]\n\t" \
"ds_read_b128 v[24:27], %[lb1] offset:13312\n\t" \
"v_fma_f32 v54, v54, v16, v16\n\t" \
"s_waitcnt lgkmcnt(4)\n\t" \
"v_mfma_f32_16x16x32_bf16 v[144:147], v[28:31], a[48:51], v[144:147]\n\t" \
"buffer_load_dwordx4 v[104:107], %[lb0], %[srd], s62 offen offset:2048\n\t" \
"v_mfma_f32_16x16x32_bf16 v[148:151], v[28:31], a[52:55], v[148:151]\n\t" \
"ds_read_b128 v[28:31], %[lb1] offset:14336\n\t" \
"v_mul_f32 v160, v160, v44\n\t" \
"v_fma_f32 v55, v55, v16, v16\n\t" \
"v_mul_f32 v161, v161, v45\n\t" \
"v_mfma_f32_16x16x32_bf16 v[152:155], v[32:35], a[48:51], v[152:155]\n\t" \
"buffer_load_dwordx4 v[108:111], %[lb0], %[srd], s62 offen offset:3072\n\t" \
"v_mfma_f32_16x16x32_bf16 v[156:159], v[32:35], a[52:55], v[156:159]\n\t" \
"ds_read_b128 v[32:35], %[lb1] offset:15360\n\t" \
"v_rcp_f32 v52, v52\n\t" \
"v_mul_f32 v162, v162, v46\n\t" \
"s_waitcnt lgkmcnt(4)\n\t" \
"v_mfma_f32_16x16x32_bf16 v[144:147], v[36:39], a[56:59], v[144:147]\n\t" \
"v_rcp_f32 v53, v53\n\t" \
"v_mfma_f32_16x16x32_bf16 v[148:151], v[36:39], a[60:63], v[148:151]\n\t" \
"ds_read_b128 v[36:39], %[lb1] offset:16384\n\t" \
"v_mul_f32 v163, v163, v47\n\t" \
"v_rcp_f32 v54, v54\n\t" \
"v_mfma_f32_16x16x32_bf16 v[152:155], v[40:43], a[56:59], v[152:155]\n\t" \
"s_add_u32 s62, %[sW2], 45056\n\t" \
"buffer_load_dwordx4 v[112:115], %[lb0], %[srd], s62 offen offset:0\n\t" \
"v_mfma_f32_16x16x32_bf16 v[156:159], v[40:43], a[60:63], v[156:159]\n\t" \
"ds_read_b128 v[40:43], %[lb1] offset:17408\n\t" \
"v_cvt_pk_bf16_f32 v168, v160, v161\n\t" \
"s_waitcnt lgkmcnt(4)\n\t" \
"v_mfma_f32_16x16x32_bf16 v[144:147], v[20:23], a[64:67], v[144:147]\n\t" \
"buffer_load_dwordx4 v[116:119], %[lb0], %[srd], s62 offen offset:1024\n\t" \
"v_mfma_f32_16x16x32_bf16 v[148:151], v[20:23], a[68:71], v[148:151]\n\t" \
"ds_read_b128 v[20:23], %[lb1] offset:18432\n\t" \
"v_rcp_f32 v55, v55\n\t" \
"v_cvt_pk_bf16_f32 v169, v162, v163\n\t" \
"v_mfma_f32_16x16x32_bf16 v[152:155], v[24:27], a[64:67], v[152:155]\n\t" \
"v_mul_f32 v164, v164, v52\n\t" \
"v_mul_f32 v44, v176, v60\n\t" \
"v_mfma_f32_16x16x32_bf16 v[156:159], v[24:27], a[68:71], v[156:159]\n\t" \
"ds_read_b128 v[24:27], %[lb1] offset:19456\n\t" \
"v_mul_f32 v165, v165, v53\n\t" \
"v_mul_f32 v45, v177, v60\n\t" \
"s_waitcnt lgkmcnt(4)\n\t" \
"v_mfma_f32_16x16x32_bf16 v[144:147], v[28:31], a[72:75], v[144:147]\n\t" \
"buffer_load_dwordx4 v[120:123], %[lb0], %[srd], s62 offen offset:2048\n\t" \
"v_mfma_f32_16x16x32_bf16 v[148:151], v[28:31], a[76:79], v[148:151]\n\t" \
"ds_read_b128 v[28:31], %[lb1] offset:20480\n\t" \
"v_mul_f32 v166, v166, v54\n\t" \
"v_mul_f32 v46, v178, v60\n\t" \
"v_mul_f32 v167, v167, v55\n\t" \
"v_mfma_f32_16x16x32_bf16 v[152:155], v[32:35], a[72:75], v[152:155]\n\t" \
"buffer_load_dwordx4 v[124:127], %[lb0], %[srd], s62 offen offset:3072\n\t" \
"v_mfma_f32_16x16x32_bf16 v[156:159], v[32:35], a[76:79], v[156:159]\n\t" \
"ds_read_b128 v[32:35], %[lb1] offset:21504\n\t" \
"v_mul_f32 v47, v179, v60\n\t" \
"v_cvt_pk_bf16_f32 v172, v164, v165\n\t" \
"v_exp_f32 v44, v44\n\t" \
"s_waitcnt lgkmcnt(4)\n\t" \
"v_mfma_f32_16x16x32_bf16 v[144:147], v[36:39], a[80:83], v[144:147]\n\t" \
"v_cvt_pk_bf16_f32 v173, v166, v167\n\t" \
"v_mul_f32 v176, v176, v184\n\t" \
"v_mfma_f32_16x16x32_bf16 v[148:151], v[36:39], a[84:87], v[148:151]\n\t" \
"ds_read_b128 v[36:39], %[lb1] offset:22528\n\t" \
"v_mul_f32 v52, v180, v62\n\t" \
"v_mfma_f32_16x16x32_bf16 v[152:155], v[40:43], a[80:83], v[152:155]\n\t" \
"s_add_u32 s62, %[sW2], 49152\n\t" \
"s_add_u32 m0, s67, 0\n\t" \
"s_nop 0\n\t" \
"buffer_load_dwordx4 %[lb0], %[srd], s62 offen offset:0 lds\n\t" \
"v_mfma_f32_16x16x32_bf16 v[156:159], v[40:43], a[84:87], v[156:159]\n\t" \
"ds_read_b128 v[40:43], %[lb1] offset:23552\n\t" \
"v_exp_f32 v45, v45\n\t" \
"s_waitcnt lgkmcnt(4)\n\t" \
"v_mfma_f32_16x16x32_bf16 v[144:147], v[20:23], a[88:91], v[144:147]\n\t" \
"buffer_load_dwordx4 %[lb0], %[srd], s62 offen offset:1024 lds\n\t" \
"v_mfma_f32_16x16x32_bf16 v[148:151], v[20:23], a[92:95], v[148:151]\n\t" \
"ds_read_b128 v[20:23], %[lb1] offset:24576\n\t" \
"v_mul_f32 v53, v181, v62\n\t" \
"v_mul_f32 v177, v177, v185\n\t" \
"v_mul_f32 v54, v182, v62\n\t" \
"v_mfma_f32_16x16x32_bf16 v[152:155], v[24:27], a[88:91], v[152:155]\n\t" \
"v_exp_f32 v46, v46\n\t" \
"v_mfma_f32_16x16x32_bf16 v[156:159], v[24:27], a[92:95], v[156:159]\n\t" \
"ds_read_b128 v[24:27], %[lb1] offset:25600\n\t" \
"v_mul_f32 v55, v183, v62\n\t" \
"v_mul_f32 v178, v178, v186\n\t" \
"s_waitcnt lgkmcnt(4)\n\t" \
"v_mfma_f32_16x16x32_bf16 v[144:147], v[28:31], a[96:99], v[144:147]\n\t" \
"buffer_load_dwordx4 %[lb0], %[srd], s62 offen offset:2048 lds\n\t" \
"v_mfma_f32_16x16x32_bf16 v[148:151], v[28:31], a[100:103], v[148:151]\n\t" \
"ds_read_b128 v[28:31], %[lb1] offset:26624\n\t" \
"v_exp_f32 v52, v52\n\t" \
"v_exp_f32 v47, v47\n\t" \
"v_mfma_f32_16x16x32_bf16 v[152:155], v[32:35], a[96:99], v[152:155]\n\t" \
"buffer_load_dwordx4 %[lb0], %[srd], s62 offen offset:3072 lds\n\t" \
"v_mfma_f32_16x16x32_bf16 v[156:159], v[32:35], a[100:103], v[156:159]\n\t" \
"ds_read_b128 v[32:35], %[lb1] offset:27648\n\t" \
"v_mul_f32 v180, v180, v188\n\t" \
"v_mul_f32 v179, v179, v187\n\t" \
"s_waitcnt lgkmcnt(4)\n\t" \
"v_mfma_f32_16x16x32_bf16 v[144:147], v[36:39], a[104:107], v[144:147]\n\t" \
"v_exp_f32 v53, v53\n\t" \
"v_mfma_f32_16x16x32_bf16 v[148:151], v[36:39], a[108:111], v[148:151]\n\t" \
"v_fma_f32 v44, v44, v14, v14\n\t" \
"v_mul_f32 v181, v181, v189\n\t" \
"v_mfma_f32_16x16x32_bf16 v[152:155], v[40:43], a[104:107], v[152:155]\n\t" \
"s_add_u32 s62, %[sW2], 53248\n\t" \
"s_add_u32 m0, s67, 4096\n\t" \
"s_nop 0\n\t" \
"buffer_load_dwordx4 %[lb0], %[srd], s62 offen offset:0 lds\n\t" \
"v_mfma_f32_16x16x32_bf16 v[156:159], v[40:43], a[108:111], v[156:159]\n\t" \
"v_fma_f32 v45, v45, v14, v14\n\t" \
"v_exp_f32 v54, v54\n\t" \
"s_waitcnt lgkmcnt(2)\n\t" \
"v_mfma_f32_16x16x32_bf16 v[144:147], v[20:23], a[112:115], v[144:147]\n\t" \
"buffer_load_dwordx4 %[lb0], %[srd], s62 offen offset:1024 lds\n\t" \
"v_mfma_f32_16x16x32_bf16 v[148:151], v[20:23], a[116:119], v[148:151]\n\t" \
"v_fma_f32 v46, v46, v14, v14\n\t" \
"v_mul_f32 v182, v182, v190\n\t" \
"v_mfma_f32_16x16x32_bf16 v[152:155], v[24:27], a[112:115], v[152:155]\n\t" \
"v_fma_f32 v47, v47, v14, v14\n\t" \
"v_exp_f32 v55, v55\n\t" \
"v_mfma_f32_16x16x32_bf16 v[156:159], v[24:27], a[116:119], v[156:159]\n\t" \
"v_rcp_f32 v44, v44\n\t" \
"s_waitcnt lgkmcnt(0)\n\t" \
"v_mfma_f32_16x16x32_bf16 v[144:147], v[28:31], a[120:123], v[144:147]\n\t" \
"buffer_load_dwordx4 %[lb0], %[srd], s62 offen offset:2048 lds\n\t" \
"v_mfma_f32_16x16x32_bf16 v[148:151], v[28:31], a[124:127], v[148:151]\n\t" \
"v_mul_f32 v183, v183, v191\n\t" \
"v_rcp_f32 v45, v45\n\t" \
"v_mfma_f32_16x16x32_bf16 v[152:155], v[32:35], a[120:123], v[152:155]\n\t" \
"buffer_load_dwordx4 %[lb0], %[srd], s62 offen offset:3072 lds\n\t" \
"v_mfma_f32_16x16x32_bf16 v[156:159], v[32:35], a[124:127], v[156:159]\n\t" \
"v_fma_f32 v52, v52, v16, v16\n\t" \
"v_rcp_f32 v46, v46\n\t" \
"v_fma_f32 v53, v53, v16, v16\n\t" \
"v_rcp_f32 v47, v47\n\t" \
"v_fma_f32 v54, v54, v16, v16\n\t" \
"v_mul_f32 v176, v176, v44\n\t" \
"v_fma_f32 v55, v55, v16, v16\n\t" \
"v_mul_f32 v177, v177, v45\n\t" \
"v_rcp_f32 v52, v52\n\t" \
"v_mul_f32 v178, v178, v46\n\t" \
"v_rcp_f32 v53, v53\n\t" \
"v_mul_f32 v179, v179, v47\n\t" \
"v_rcp_f32 v54, v54\n\t" \
"v_cvt_pk_bf16_f32 v170, v176, v177\n\t" \
"v_rcp_f32 v55, v55\n\t" \
"v_cvt_pk_bf16_f32 v171, v178, v179\n\t" \
"v_mul_f32 v180, v180, v52\n\t" \
"s_add_u32 s66, %[sH2s], 1048576\n\t" \
"buffer_store_dwordx4 v[168:171], %[lb0], %[srd], s66 offen sc1\n\t" \
"v_mul_f32 v181, v181, v53\n\t" \
"v_mul_f32 v182, v182, v54\n\t" \
"v_mul_f32 v183, v183, v55\n\t" \
"v_cvt_pk_bf16_f32 v174, v180, v181\n\t" \
"v_cvt_pk_bf16_f32 v175, v182, v183\n\t" \
"buffer_store_dwordx4 v[172:175], %[lb0], %[srd], s66 offen offset:1024 sc1\n\t" \
"v_pk_mul_f32 v[52:53], v[148:149], v[62:63]\n\t" \
"v_pk_mul_f32 v[44:45], v[144:145], v[60:61]\n\t" \
"v_pk_mul_f32 v[54:55], v[150:151], v[62:63]\n\t" \
"v_pk_mul_f32 v[46:47], v[146:147], v[60:61]\n\t" \
"v_pk_mul_f32 v[148:149], v[148:149], v[156:157]\n\t" \
"v_pk_mul_f32 v[144:145], v[144:145], v[152:153]\n\t" \
"v_pk_mul_f32 v[150:151], v[150:151], v[158:159]\n\t" \
"v_pk_mul_f32 v[146:147], v[146:147], v[154:155]\n\t" \
"v_exp_f32 v52, v52\n\t" \
"v_exp_f32 v44, v44\n\t" \
"v_exp_f32 v53, v53\n\t" \
"v_exp_f32 v45, v45\n\t" \
"v_exp_f32 v54, v54\n\t" \
"v_exp_f32 v46, v46\n\t" \
"v_exp_f32 v55, v55\n\t" \
"v_exp_f32 v47, v47\n\t" \
"v_pk_fma_f32 v[52:53], v[52:53], v[16:17], v[16:17]\n\t" \
"v_pk_fma_f32 v[44:45], v[44:45], v[14:15], v[14:15]\n\t" \
"v_pk_fma_f32 v[54:55], v[54:55], v[16:17], v[16:17]\n\t" \
"v_pk_fma_f32 v[46:47], v[46:47], v[14:15], v[14:15]\n\t" \
"v_rcp_f32 v52, v52\n\t" \
"v_rcp_f32 v44, v44\n\t" \
"v_rcp_f32 v53, v53\n\t" \
"v_rcp_f32 v45, v45\n\t" \
"v_rcp_f32 v54, v54\n\t" \
"v_rcp_f32 v46, v46\n\t" \
"v_rcp_f32 v55, v55\n\t" \
"v_rcp_f32 v47, v47\n\t" \
"v_pk_mul_f32 v[148:149], v[148:149], v[52:53]\n\t" \
"v_pk_mul_f32 v[144:145], v[144:145], v[44:45]\n\t" \
"v_pk_mul_f32 v[150:151], v[150:151], v[54:55]\n\t" \
"v_pk_mul_f32 v[146:147], v[146:147], v[46:47]\n\t" \
"v_cvt_pk_bf16_f32 v154, v148, v149\n\t" \
"v_cvt_pk_bf16_f32 v152, v144, v145\n\t" \
"v_cvt_pk_bf16_f32 v155, v150, v151\n\t" \
"v_cvt_pk_bf16_f32 v153, v146, v147\n\t" \
"v_permlane16_swap_b32 v152, v154\n\t" \
"s_nop 0\n\t" \
"v_permlane16_swap_b32 v153, v155\n\t" \
"buffer_store_dwordx4 v[152:155], %[tv], %[srd], %[sH2t] offen sc1\n\t" \
"v_bfe_u32 v26, %[lb0], 4, 2\n\t" \
"v_lshlrev_b32 v26, 2, v26\n\t" \
"buffer_load_dword v28, v26, %[srd], %[sFp] offen offset:16 sc1\n\t" \
"s_add_u32 s64, %[w1], 0\n\t" \
"s_and_b32 s64, s64, 0xc00\n\t" \
"s_mul_i32 s64, s64, 12\n\t" \
"v_add_u32 v20, s64, %[lb0]\n\t" \
"s_add_u32 s64, %[w1], 1024\n\t" \
"s_and_b32 s64, s64, 0xc00\n\t" \
"s_mul_i32 s64, s64, 12\n\t" \
"v_add_u32 v21, s64, %[lb0]\n\t" \
"s_add_u32 s64, %[w1], 2048\n\t" \
"s_and_b32 s64, s64, 0xc00\n\t" \
"s_mul_i32 s64, s64, 12\n\t" \
"s_add_u32 s64, s64, 4096\n\t" \
"v_add_u32 v22, s64, %[lb0]\n\t" \
"s_add_u32 s64, %[w1], 3072\n\t" \
"s_and_b32 s64, s64, 0xc00\n\t" \
"s_mul_i32 s64, s64, 12\n\t" \
"s_add_u32 s64, s64, 8192\n\t" \
"v_add_u32 v23, s64, %[lb0]\n\t" \
"v_lshrrev_b32 v25, 2, %[lb0]\n\t" \
"v_and_b32 v25, 0xc0, v25\n\t" \
"v_add_u32 v27, s67, %[lb0]\n\t" \
"s_mov_b32 s63, 0\n\t" \
"s_waitcnt vmcnt(0)\n\t" \
"s_barrier\n\t" \
"s_mov_b64 exec, s[70:71]\n\t" \
"buffer_store_dword v18, off, %[srd], %[sFs] sc1\n\t" \
"s_mov_b64 exec, -1\n\t" \
"v_cmp_ne_u32 vcc, 1, v28\n\t" \
"s_cbranch_vccnz Lps%=\n\t" \
"Lpk%=:\n\t" \
"s_mov_b32 s60, %[sHr]\n\t" \
"buffer_load_dwordx4 a[0:3], %[hv0], %[srd], s60 offen sc1\n\t" \
"buffer_load_dwordx4 a[4:7], %[hv1], %[srd], s60 offen sc1\n\t" \
"buffer_load_dwordx4 a[8:11], %[hv2], %[srd], s60 offen sc1\n\t" \
"buffer_load_dwordx4 a[12:15], %[hv3], %[srd], s60 offen sc1\n\t" \
"s_add_u32 s60, s60, 0x20000\n\t" \
"s_cmp_eq_u32 s60, s68\n\t" \
"s_cselect_b32 s60, %[sHl], s60\n\t" \
"buffer_load_dwordx4 a[16:19], %[hv0], %[srd], s60 offen sc1\n\t" \
"buffer_load_dwordx4 a[20:23], %[hv1], %[srd], s60 offen sc1\n\t" \
"buffer_load_dwordx4 a[24:27], %[hv2], %[srd], s60 offen sc1\n\t" \
"buffer_load_dwordx4 a[28:31], %[hv3], %[srd], s60 offen sc1\n\t" \
"s_add_u32 s60, s60, 0x20000\n\t" \
"s_cmp_eq_u32 s60, s68\n\t" \
"s_cselect_b32 s60, %[sHl], s60\n\t" \
"buffer_load_dwordx4 a[32:35], %[hv0], %[srd], s60 offen sc1\n\t" \
"buffer_load_dwordx4 a[36:39], %[hv1], %[srd], s60 offen sc1\n\t" \
"buffer_load_dwordx4 a[40:43], %[hv2], %[srd], s60 offen sc1\n\t" \
"buffer_load_dwordx4 a[44:47], %[hv3], %[srd], s60 offen sc1\n\t" \
"ds_read_b128 v[128:131], v27 offset:0\n\t" \
"ds_read_b128 v[132:135], v27 offset:1024\n\t" \
"ds_read_b128 v[136:139], v27 offset:2048\n\t" \
"ds_read_b128 v[140:143], v27 offset:3072\n\t" \
"ds_read_b128 v[160:163], v27 offset:4096\n\t" \
"ds_read_b128 v[164:167], v27 offset:5120\n\t" \
"ds_read_b128 v[168:171], v27 offset:6144\n\t" \
"ds_read_b128 v[172:175], v27 offset:7168\n\t" \
"s_waitcnt lgkmcnt(0)\n\t" \
"s_waitcnt vmcnt(11)\n\t" \
"v_mfma_f32_16x16x32_bf16 v[192:195], a[128:131], a[0:3], 0\n\t" \
"s_add_u32 s60, s60, 0x20000\n\t" \
"s_cmp_eq_u32 s60, s68\n\t" \
"s_cselect_b32 s60, %[sHl], s60\n\t" \
"buffer_load_dwordx4 a[48:51], %[hv0], %[srd], s60 offen sc1\n\t" \
"v_mfma_f32_16x16x32_bf16 v[196:199], a[132:135], a[0:3], 0\n\t" \
"v_mfma_f32_16x16x32_bf16 v[200:203], a[136:139], a[0:3], 0\n\t" \
"v_mfma_f32_16x16x32_bf16 v[204:207], a[140:143], a[0:3], 0\n\t" \
"s_waitcnt vmcnt(11)\n\t" \
"v_mfma_f32_16x16x32_bf16 v[208:211], a[128:131], a[4:7], 0\n\t" \
"buffer_load_dwordx4 a[52:55], %[hv1], %[srd], s60 offen sc1\n\t" \
"v_mfma_f32_16x16x32_bf16 v[212:215], a[132:135], a[4:7], 0\n\t" \
"v_mfma_f32_16x16x32_bf16 v[216:219], a[136:139], a[4:7], 0\n\t" \
"v_mfma_f32_16x16x32_bf16 v[220:223], a[140:143], a[4:7], 0\n\t" \
"s_waitcnt vmcnt(11)\n\t" \
"v_mfma_f32_16x16x32_bf16 v[224:227], a[128:131], a[8:11], 0\n\t" \
"buffer_load_dwordx4 a[56:59], %[hv2], %[srd], s60 offen sc1\n\t" \
"v_mfma_f32_16x16x32_bf16 v[228:231], a[132:135], a[8:11], 0\n\t" \
"v_mfma_f32_16x16x32_bf16 v[232:235], a[136:139], a[8:11], 0\n\t" \
"v_mfma_f32_16x16x32_bf16 v[236:239], a[140:143], a[8:11], 0\n\t" \
"s_waitcnt vmcnt(11)\n\t" \
"v_mfma_f32_16x16x32_bf16 v[240:243], a[128:131], a[12:15], 0\n\t" \
"buffer_load_dwordx4 a[60:63], %[hv3], %[srd], s60 offen sc1\n\t" \
"v_mfma_f32_16x16x32_bf16 v[244:247], a[132:135], a[12:15], 0\n\t" \
"v_mfma_f32_16x16x32_bf16 v[248:251], a[136:139], a[12:15], 0\n\t" \
"v_mfma_f32_16x16x32_bf16 v[252:255], a[140:143], a[12:15], 0\n\t" \
"s_waitcnt vmcnt(11)\n\t" \
"v_mfma_f32_16x16x32_bf16 v[192:195], a[144:147], a[16:19], v[192:195]\n\t" \
"s_add_u32 s60, s60, 0x20000\n\t" \
"s_cmp_eq_u32 s60, s68\n\t" \
"s_cselect_b32 s60, %[sHl], s60\n\t" \
"buffer_load_dwordx4 a[64:67], %[hv0], %[srd], s60 offen sc1\n\t" \
"v_mfma_f32_16x16x32_bf16 v[196:199], a[148:151], a[16:19], v[196:199]\n\t" \
"v_mfma_f32_16x16x32_bf16 v[200:203], a[152:155], a[16:19], v[200:203]\n\t" \
"v_mfma_f32_16x16x32_bf16 v[204:207], a[156:159], a[16:19], v[204:207]\n\t" \
"s_waitcnt vmcnt(11)\n\t" \
"v_mfma_f32_16x16x32_bf16 v[208:211], a[144:147], a[20:23], v[208:211]\n\t" \
"buffer_load_dwordx4 a[68:71], %[hv1], %[srd], s60 offen sc1\n\t" \
"v_mfma_f32_16x16x32_bf16 v[212:215], a[148:151], a[20:23], v[212:215]\n\t" \
"v_mfma_f32_16x16x32_bf16 v[216:219], a[152:155], a[20:23], v[216:219]\n\t" \
"v_mfma_f32_16x16x32_bf16 v[220:223], a[156:159], a[20:23], v[220:223]\n\t" \
"s_waitcnt vmcnt(11)\n\t" \
"v_mfma_f32_16x16x32_bf16 v[224:227], a[144:147], a[24:27], v[224:227]\n\t" \
"buffer_load_dwordx4 a[72:75], %[hv2], %[srd], s60 offen sc1\n\t" \
"v_mfma_f32_16x16x32_bf16 v[228:231], a[148:151], a[24:27], v[228:231]\n\t" \
"v_mfma_f32_16x16x32_bf16 v[232:235], a[152:155], a[24:27], v[232:235]\n\t" \
"v_mfma_f32_16x16x32_bf16 v[236:239], a[156:159], a[24:27], v[236:239]\n\t" \
"s_waitcnt vmcnt(11)\n\t" \
"v_mfma_f32_16x16x32_bf16 v[240:243], a[144:147], a[28:31], v[240:243]\n\t" \
"buffer_load_dwordx4 a[76:79], %[hv3], %[srd], s60 offen sc1\n\t" \
"v_mfma_f32_16x16x32_bf16 v[244:247], a[148:151], a[28:31], v[244:247]\n\t" \
"v_mfma_f32_16x16x32_bf16 v[248:251], a[152:155], a[28:31], v[248:251]\n\t" \
"v_mfma_f32_16x16x32_bf16 v[252:255], a[156:159], a[28:31], v[252:255]\n\t" \
"s_waitcnt vmcnt(11)\n\t" \
"v_mfma_f32_16x16x32_bf16 v[192:195], a[160:163], a[32:35], v[192:195]\n\t" \
"buffer_load_dword v29, v26, %[srd], %[sFp] offen sc1\n\t" \
"s_add_u32 s60, s60, 0x20000\n\t" \
"s_cmp_eq_u32 s60, s68\n\t" \
"s_cselect_b32 s60, %[sHl], s60\n\t" \
"buffer_load_dwordx4 a[80:83], %[hv0], %[srd], s60 offen sc1\n\t" \
"v_mfma_f32_16x16x32_bf16 v[196:199], a[164:167], a[32:35], v[196:199]\n\t" \
"v_mfma_f32_16x16x32_bf16 v[200:203], a[168:171], a[32:35], v[200:203]\n\t" \
"v_mfma_f32_16x16x32_bf16 v[204:207], a[172:175], a[32:35], v[204:207]\n\t" \
"s_waitcnt vmcnt(12)\n\t" \
"v_mfma_f32_16x16x32_bf16 v[208:211], a[160:163], a[36:39], v[208:211]\n\t" \
"buffer_load_dwordx4 a[84:87], %[hv1], %[srd], s60 offen sc1\n\t" \
"v_mfma_f32_16x16x32_bf16 v[212:215], a[164:167], a[36:39], v[212:215]\n\t" \
"v_mfma_f32_16x16x32_bf16 v[216:219], a[168:171], a[36:39], v[216:219]\n\t" \
"v_mfma_f32_16x16x32_bf16 v[220:223], a[172:175], a[36:39], v[220:223]\n\t" \
"s_waitcnt vmcnt(12)\n\t" \
"v_mfma_f32_16x16x32_bf16 v[224:227], a[160:163], a[40:43], v[224:227]\n\t" \
"buffer_load_dwordx4 a[88:91], %[hv2], %[srd], s60 offen sc1\n\t" \
"v_mfma_f32_16x16x32_bf16 v[228:231], a[164:167], a[40:43], v[228:231]\n\t" \
"v_mfma_f32_16x16x32_bf16 v[232:235], a[168:171], a[40:43], v[232:235]\n\t" \
"v_mfma_f32_16x16x32_bf16 v[236:239], a[172:175], a[40:43], v[236:239]\n\t" \
"s_waitcnt vmcnt(12)\n\t" \
"v_mfma_f32_16x16x32_bf16 v[240:243], a[160:163], a[44:47], v[240:243]\n\t" \
"buffer_load_dwordx4 a[92:95], %[hv3], %[srd], s60 offen sc1\n\t" \
"v_mfma_f32_16x16x32_bf16 v[244:247], a[164:167], a[44:47], v[244:247]\n\t" \
"v_mfma_f32_16x16x32_bf16 v[248:251], a[168:171], a[44:47], v[248:251]\n\t" \
"v_mfma_f32_16x16x32_bf16 v[252:255], a[172:175], a[44:47], v[252:255]\n\t" \
"s_waitcnt vmcnt(12)\n\t" \
"v_mfma_f32_16x16x32_bf16 v[192:195], a[176:179], a[48:51], v[192:195]\n\t" \
"s_add_u32 s60, s60, 0x20000\n\t" \
"s_cmp_eq_u32 s60, s68\n\t" \
"s_cselect_b32 s60, %[sHl], s60\n\t" \
"buffer_load_dwordx4 a[96:99], %[hv0], %[srd], s60 offen sc1\n\t" \
"v_mfma_f32_16x16x32_bf16 v[196:199], a[180:183], a[48:51], v[196:199]\n\t" \
"v_mfma_f32_16x16x32_bf16 v[200:203], a[184:187], a[48:51], v[200:203]\n\t" \
"v_mfma_f32_16x16x32_bf16 v[204:207], a[188:191], a[48:51], v[204:207]\n\t" \
"s_waitcnt vmcnt(12)\n\t" \
"v_mfma_f32_16x16x32_bf16 v[208:211], a[176:179], a[52:55], v[208:211]\n\t" \
"buffer_load_dwordx4 a[100:103], %[hv1], %[srd], s60 offen sc1\n\t" \
"v_mfma_f32_16x16x32_bf16 v[212:215], a[180:183], a[52:55], v[212:215]\n\t" \
"v_mfma_f32_16x16x32_bf16 v[216:219], a[184:187], a[52:55], v[216:219]\n\t" \
"v_mfma_f32_16x16x32_bf16 v[220:223], a[188:191], a[52:55], v[220:223]\n\t" \
"s_waitcnt vmcnt(12)\n\t" \
"v_mfma_f32_16x16x32_bf16 v[224:227], a[176:179], a[56:59], v[224:227]\n\t" \
"buffer_load_dwordx4 a[104:107], %[hv2], %[srd], s60 offen sc1\n\t" \
"v_mfma_f32_16x16x32_bf16 v[228:231], a[180:183], a[56:59], v[228:231]\n\t" \
"v_mfma_f32_16x16x32_bf16 v[232:235], a[184:187], a[56:59], v[232:235]\n\t" \
"v_mfma_f32_16x16x32_bf16 v[236:239], a[188:191], a[56:59], v[236:239]\n\t" \
"s_waitcnt vmcnt(12)\n\t" \
"v_mfma_f32_16x16x32_bf16 v[240:243], a[176:179], a[60:63], v[240:243]\n\t" \
"buffer_load_dwordx4 a[108:111], %[hv3], %[srd], s60 offen sc1\n\t" \
"v_mfma_f32_16x16x32_bf16 v[244:247], a[180:183], a[60:63], v[244:247]\n\t" \
"v_mfma_f32_16x16x32_bf16 v[248:251], a[184:187], a[60:63], v[248:251]\n\t" \
"v_mfma_f32_16x16x32_bf16 v[252:255], a[188:191], a[60:63], v[252:255]\n\t" \
"s_waitcnt vmcnt(12)\n\t" \
"v_mfma_f32_16x16x32_bf16 v[192:195], a[192:195], a[64:67], v[192:195]\n\t" \
"s_add_u32 s60, s60, 0x20000\n\t" \
"s_cmp_eq_u32 s60, s68\n\t" \
"s_cselect_b32 s60, %[sHl], s60\n\t" \
"buffer_load_dwordx4 a[112:115], %[hv0], %[srd], s60 offen sc1\n\t" \
"v_mfma_f32_16x16x32_bf16 v[196:199], a[196:199], a[64:67], v[196:199]\n\t" \
"v_mfma_f32_16x16x32_bf16 v[200:203], a[200:203], a[64:67], v[200:203]\n\t" \
"v_mfma_f32_16x16x32_bf16 v[204:207], a[204:207], a[64:67], v[204:207]\n\t" \
"s_waitcnt vmcnt(12)\n\t" \
"v_mfma_f32_16x16x32_bf16 v[208:211], a[192:195], a[68:71], v[208:211]\n\t" \
"buffer_load_dwordx4 a[116:119], %[hv1], %[srd], s60 offen sc1\n\t" \
"v_mfma_f32_16x16x32_bf16 v[212:215], a[196:199], a[68:71], v[212:215]\n\t" \
"v_mfma_f32_16x16x32_bf16 v[216:219], a[200:203], a[68:71], v[216:219]\n\t" \
"v_mfma_f32_16x16x32_bf16 v[220:223], a[204:207], a[68:71], v[220:223]\n\t" \
"s_waitcnt vmcnt(12)\n\t" \
"v_mfma_f32_16x16x32_bf16 v[224:227], a[192:195], a[72:75], v[224:227]\n\t" \
"buffer_load_dwordx4 a[120:123], %[hv2], %[srd], s60 offen sc1\n\t" \
"v_mfma_f32_16x16x32_bf16 v[228:231], a[196:199], a[72:75], v[228:231]\n\t" \
"v_mfma_f32_16x16x32_bf16 v[232:235], a[200:203], a[72:75], v[232:235]\n\t" \
"v_mfma_f32_16x16x32_bf16 v[236:239], a[204:207], a[72:75], v[236:239]\n\t" \
"s_waitcnt vmcnt(12)\n\t" \
"v_mfma_f32_16x16x32_bf16 v[240:243], a[192:195], a[76:79], v[240:243]\n\t" \
"buffer_load_dwordx4 a[124:127], %[hv3], %[srd], s60 offen sc1\n\t" \
"v_mfma_f32_16x16x32_bf16 v[244:247], a[196:199], a[76:79], v[244:247]\n\t" \
"v_mfma_f32_16x16x32_bf16 v[248:251], a[200:203], a[76:79], v[248:251]\n\t" \
"v_mfma_f32_16x16x32_bf16 v[252:255], a[204:207], a[76:79], v[252:255]\n\t" \
"s_waitcnt vmcnt(11)\n\t" \
"v_mfma_f32_16x16x32_bf16 v[192:195], a[208:211], a[80:83], v[192:195]\n\t" \
"v_cmp_ne_u32 vcc, 1, v29\n\t" \
"s_cbranch_vccnz Lqs%=\n\t" \
"Lqk%=:\n\t" \
"s_mov_b32 s60, %[sHr2]\n\t" \
"buffer_load_dwordx4 a[0:3], %[hv0], %[srd], s60 offen sc1\n\t" \
"v_mfma_f32_16x16x32_bf16 v[196:199], a[212:215], a[80:83], v[196:199]\n\t" \
"v_mfma_f32_16x16x32_bf16 v[200:203], a[216:219], a[80:83], v[200:203]\n\t" \
"v_mfma_f32_16x16x32_bf16 v[204:207], a[220:223], a[80:83], v[204:207]\n\t" \
"s_waitcnt vmcnt(11)\n\t" \
"v_mfma_f32_16x16x32_bf16 v[208:211], a[208:211], a[84:87], v[208:211]\n\t" \
"buffer_load_dwordx4 a[4:7], %[hv1], %[srd], s60 offen sc1\n\t" \
"v_mfma_f32_16x16x32_bf16 v[212:215], a[212:215], a[84:87], v[212:215]\n\t" \
"v_mfma_f32_16x16x32_bf16 v[216:219], a[216:219], a[84:87], v[216:219]\n\t" \
"v_mfma_f32_16x16x32_bf16 v[220:223], a[220:223], a[84:87], v[220:223]\n\t" \
"s_waitcnt vmcnt(11)\n\t" \
"v_mfma_f32_16x16x32_bf16 v[224:227], a[208:211], a[88:91], v[224:227]\n\t" \
"buffer_load_dwordx4 a[8:11], %[hv2], %[srd], s60 offen sc1\n\t" \
"v_mfma_f32_16x16x32_bf16 v[228:231], a[212:215], a[88:91], v[228:231]\n\t" \
"v_mfma_f32_16x16x32_bf16 v[232:235], a[216:219], a[88:91], v[232:235]\n\t" \
"v_mfma_f32_16x16x32_bf16 v[236:239], a[220:223], a[88:91], v[236:239]\n\t" \
"s_waitcnt vmcnt(11)\n\t" \
"v_mfma_f32_16x16x32_bf16 v[240:243], a[208:211], a[92:95], v[240:243]\n\t" \
"buffer_load_dwordx4 a[12:15], %[hv3], %[srd], s60 offen sc1\n\t" \
"v_mfma_f32_16x16x32_bf16 v[244:247], a[212:215], a[92:95], v[244:247]\n\t" \
"v_mfma_f32_16x16x32_bf16 v[248:251], a[216:219], a[92:95], v[248:251]\n\t" \
"v_mfma_f32_16x16x32_bf16 v[252:255], a[220:223], a[92:95], v[252:255]\n\t" \
"s_waitcnt vmcnt(11)\n\t" \
"v_mfma_f32_16x16x32_bf16 v[192:195], a[224:227], a[96:99], v[192:195]\n\t" \
"s_add_u32 s60, s60, 0x20000\n\t" \
"s_cmp_eq_u32 s60, s69\n\t" \
"s_cselect_b32 s60, s68, s60\n\t" \
"buffer_load_dwordx4 a[16:19], %[hv0], %[srd], s60 offen sc1\n\t" \
"v_mfma_f32_16x16x32_bf16 v[196:199], a[228:231], a[96:99], v[196:199]\n\t" \
"v_mfma_f32_16x16x32_bf16 v[200:203], a[232:235], a[96:99], v[200:203]\n\t" \
"v_mfma_f32_16x16x32_bf16 v[204:207], a[236:239], a[96:99], v[204:207]\n\t" \
"s_waitcnt vmcnt(11)\n\t" \
"v_mfma_f32_16x16x32_bf16 v[208:211], a[224:227], a[100:103], v[208:211]\n\t" \
"buffer_load_dwordx4 a[20:23], %[hv1], %[srd], s60 offen sc1\n\t" \
"v_mfma_f32_16x16x32_bf16 v[212:215], a[228:231], a[100:103], v[212:215]\n\t" \
"v_mfma_f32_16x16x32_bf16 v[216:219], a[232:235], a[100:103], v[216:219]\n\t" \
"v_mfma_f32_16x16x32_bf16 v[220:223], a[236:239], a[100:103], v[220:223]\n\t" \
"s_waitcnt vmcnt(11)\n\t" \
"v_mfma_f32_16x16x32_bf16 v[224:227], a[224:227], a[104:107], v[224:227]\n\t" \
"buffer_load_dwordx4 a[24:27], %[hv2], %[srd], s60 offen sc1\n\t" \
"v_mfma_f32_16x16x32_bf16 v[228:231], a[228:231], a[104:107], v[228:231]\n\t" \
"v_mfma_f32_16x16x32_bf16 v[232:235], a[232:235], a[104:107], v[232:235]\n\t" \
"v_mfma_f32_16x16x32_bf16 v[236:239], a[236:239], a[104:107], v[236:239]\n\t" \
"s_waitcnt vmcnt(11)\n\t" \
"v_mfma_f32_16x16x32_bf16 v[240:243], a[224:227], a[108:111], v[240:243]\n\t" \
"buffer_load_dwordx4 a[28:31], %[hv3], %[srd], s60 offen sc1\n\t" \
"v_mfma_f32_16x16x32_bf16 v[244:247], a[228:231], a[108:111], v[244:247]\n\t" \
"v_mfma_f32_16x16x32_bf16 v[248:251], a[232:235], a[108:111], v[248:251]\n\t" \
"v_mfma_f32_16x16x32_bf16 v[252:255], a[236:239], a[108:111], v[252:255]\n\t" \
"s_waitcnt vmcnt(11)\n\t" \
"v_mfma_f32_16x16x32_bf16 v[192:195], a[240:243], a[112:115], v[192:195]\n\t" \
"s_add_u32 s60, s60, 0x20000\n\t" \
"s_cmp_eq_u32 s60, s69\n\t" \
"s_cselect_b32 s60, s68, s60\n\t" \
"buffer_load_dwordx4 a[32:35], %[hv0], %[srd], s60 offen sc1\n\t" \
"v_mfma_f32_16x16x32_bf16 v[196:199], a[244:247], a[112:115], v[196:199]\n\t" \
"v_mfma_f32_16x16x32_bf16 v[200:203], a[248:251], a[112:115], v[200:203]\n\t" \
"v_mfma_f32_16x16x32_bf16 v[204:207], a[252:255], a[112:115], v[204:207]\n\t" \
"s_waitcnt vmcnt(11)\n\t" \
"v_mfma_f32_16x16x32_bf16 v[208:211], a[240:243], a[116:119], v[208:211]\n\t" \
"buffer_load_dwordx4 a[36:39], %[hv1], %[srd], s60 offen sc1\n\t" \
"v_mfma_f32_16x16x32_bf16 v[212:215], a[244:247], a[116:119], v[212:215]\n\t" \
"v_mfma_f32_16x16x32_bf16 v[216:219], a[248:251], a[116:119], v[216:219]\n\t" \
"v_mfma_f32_16x16x32_bf16 v[220:223], a[252:255], a[116:119], v[220:223]\n\t" \
"s_waitcnt vmcnt(11)\n\t" \
"v_mfma_f32_16x16x32_bf16 v[224:227], a[240:243], a[120:123], v[224:227]\n\t" \
"buffer_load_dwordx4 a[40:43], %[hv2], %[srd], s60 offen sc1\n\t" \
"v_mfma_f32_16x16x32_bf16 v[228:231], a[244:247], a[120:123], v[228:231]\n\t" \
"v_mfma_f32_16x16x32_bf16 v[232:235], a[248:251], a[120:123], v[232:235]\n\t" \
"v_mfma_f32_16x16x32_bf16 v[236:239], a[252:255], a[120:123], v[236:239]\n\t" \
"s_waitcnt vmcnt(11)\n\t" \
"v_mfma_f32_16x16x32_bf16 v[240:243], a[240:243], a[124:127], v[240:243]\n\t" \
"buffer_load_dwordx4 a[44:47], %[hv3], %[srd], s60 offen sc1\n\t" \
"v_mfma_f32_16x16x32_bf16 v[244:247], a[244:247], a[124:127], v[244:247]\n\t" \
"v_mfma_f32_16x16x32_bf16 v[248:251], a[248:251], a[124:127], v[248:251]\n\t" \
"v_mfma_f32_16x16x32_bf16 v[252:255], a[252:255], a[124:127], v[252:255]\n\t" \
"s_waitcnt vmcnt(11)\n\t" \
"v_mfma_f32_16x16x32_bf16 v[192:195], v[64:67], a[0:3], v[192:195]\n\t" \
"s_add_u32 s60, s60, 0x20000\n\t" \
"s_cmp_eq_u32 s60, s69\n\t" \
"s_cselect_b32 s60, s68, s60\n\t" \
"buffer_load_dwordx4 a[48:51], %[hv0], %[srd], s60 offen sc1\n\t" \
"v_mfma_f32_16x16x32_bf16 v[196:199], v[68:71], a[0:3], v[196:199]\n\t" \
"v_mfma_f32_16x16x32_bf16 v[200:203], v[72:75], a[0:3], v[200:203]\n\t" \
"v_mfma_f32_16x16x32_bf16 v[204:207], v[76:79], a[0:3], v[204:207]\n\t" \
"s_waitcnt vmcnt(11)\n\t" \
"v_mfma_f32_16x16x32_bf16 v[208:211], v[64:67], a[4:7], v[208:211]\n\t" \
"buffer_load_dwordx4 a[52:55], %[hv1], %[srd], s60 offen sc1\n\t" \
"v_mfma_f32_16x16x32_bf16 v[212:215], v[68:71], a[4:7], v[212:215]\n\t" \
"v_mfma_f32_16x16x32_bf16 v[216:219], v[72:75], a[4:7], v[216:219]\n\t" \
"v_mfma_f32_16x16x32_bf16 v[220:223], v[76:79], a[4:7], v[220:223]\n\t" \
"s_waitcnt vmcnt(11)\n\t" \
"v_mfma_f32_16x16x32_bf16 v[224:227], v[64:67], a[8:11], v[224:227]\n\t" \
"buffer_load_dwordx4 a[56:59], %[hv2], %[srd], s60 offen sc1\n\t" \
"v_mfma_f32_16x16x32_bf16 v[228:231], v[68:71], a[8:11], v[228:231]\n\t" \
"v_mfma_f32_16x16x32_bf16 v[232:235], v[72:75], a[8:11], v[232:235]\n\t" \
"v_mfma_f32_16x16x32_bf16 v[236:239], v[76:79], a[8:11], v[236:239]\n\t" \
"s_waitcnt vmcnt(11)\n\t" \
"v_mfma_f32_16x16x32_bf16 v[240:243], v[64:67], a[12:15], v[240:243]\n\t" \
"buffer_load_dwordx4 a[60:63], %[hv3], %[srd], s60 offen sc1\n\t" \
"v_mfma_f32_16x16x32_bf16 v[244:247], v[68:71], a[12:15], v[244:247]\n\t" \
"v_mfma_f32_16x16x32_bf16 v[248:251], v[72:75], a[12:15], v[248:251]\n\t" \
"v_mfma_f32_16x16x32_bf16 v[252:255], v[76:79], a[12:15], v[252:255]\n\t" \
"s_waitcnt vmcnt(11)\n\t" \
"v_mfma_f32_16x16x32_bf16 v[192:195], v[80:83], a[16:19], v[192:195]\n\t" \
"s_add_u32 s60, s60, 0x20000\n\t" \
"s_cmp_eq_u32 s60, s69\n\t" \
"s_cselect_b32 s60, s68, s60\n\t" \
"buffer_load_dwordx4 a[64:67], %[hv0], %[srd], s60 offen sc1\n\t" \
"v_mfma_f32_16x16x32_bf16 v[196:199], v[84:87], a[16:19], v[196:199]\n\t" \
"v_mfma_f32_16x16x32_bf16 v[200:203], v[88:91], a[16:19], v[200:203]\n\t" \
"v_mfma_f32_16x16x32_bf16 v[204:207], v[92:95], a[16:19], v[204:207]\n\t" \
"s_waitcnt vmcnt(11)\n\t" \
"v_mfma_f32_16x16x32_bf16 v[208:211], v[80:83], a[20:23], v[208:211]\n\t" \
"buffer_load_dwordx4 a[68:71], %[hv1], %[srd], s60 offen sc1\n\t" \
"v_mfma_f32_16x16x32_bf16 v[212:215], v[84:87], a[20:23], v[212:215]\n\t" \
"v_mfma_f32_16x16x32_bf16 v[216:219], v[88:91], a[20:23], v[216:219]\n\t" \
"v_mfma_f32_16x16x32_bf16 v[220:223], v[92:95], a[20:23], v[220:223]\n\t" \
"s_waitcnt vmcnt(11)\n\t" \
"v_mfma_f32_16x16x32_bf16 v[224:227], v[80:83], a[24:27], v[224:227]\n\t" \
"buffer_load_dwordx4 a[72:75], %[hv2], %[srd], s60 offen sc1\n\t" \
"v_mfma_f32_16x16x32_bf16 v[228:231], v[84:87], a[24:27], v[228:231]\n\t" \
"v_mfma_f32_16x16x32_bf16 v[232:235], v[88:91], a[24:27], v[232:235]\n\t" \
"v_mfma_f32_16x16x32_bf16 v[236:239], v[92:95], a[24:27], v[236:239]\n\t" \
"s_waitcnt vmcnt(11)\n\t" \
"v_mfma_f32_16x16x32_bf16 v[240:243], v[80:83], a[28:31], v[240:243]\n\t" \
"buffer_load_dwordx4 a[76:79], %[hv3], %[srd], s60 offen sc1\n\t" \
"v_mfma_f32_16x16x32_bf16 v[244:247], v[84:87], a[28:31], v[244:247]\n\t" \
"v_mfma_f32_16x16x32_bf16 v[248:251], v[88:91], a[28:31], v[248:251]\n\t" \
"v_mfma_f32_16x16x32_bf16 v[252:255], v[92:95], a[28:31], v[252:255]\n\t" \
"s_waitcnt vmcnt(11)\n\t" \
"v_mfma_f32_16x16x32_bf16 v[192:195], v[96:99], a[32:35], v[192:195]\n\t" \
"s_add_u32 s60, s60, 0x20000\n\t" \
"s_cmp_eq_u32 s60, s69\n\t" \
"s_cselect_b32 s60, s68, s60\n\t" \
"buffer_load_dwordx4 a[80:83], %[hv0], %[srd], s60 offen sc1\n\t" \
"v_mfma_f32_16x16x32_bf16 v[196:199], v[100:103], a[32:35], v[196:199]\n\t" \
"v_mfma_f32_16x16x32_bf16 v[200:203], v[104:107], a[32:35], v[200:203]\n\t" \
"v_mfma_f32_16x16x32_bf16 v[204:207], v[108:111], a[32:35], v[204:207]\n\t" \
"s_waitcnt vmcnt(11)\n\t" \
"v_mfma_f32_16x16x32_bf16 v[208:211], v[96:99], a[36:39], v[208:211]\n\t" \
"buffer_load_dwordx4 a[84:87], %[hv1], %[srd], s60 offen sc1\n\t" \
"v_mfma_f32_16x16x32_bf16 v[212:215], v[100:103], a[36:39], v[212:215]\n\t" \
"v_mfma_f32_16x16x32_bf16 v[216:219], v[104:107], a[36:39], v[216:219]\n\t" \
"v_mfma_f32_16x16x32_bf16 v[220:223], v[108:111], a[36:39], v[220:223]\n\t" \
"s_waitcnt vmcnt(11)\n\t" \
"v_mfma_f32_16x16x32_bf16 v[224:227], v[96:99], a[40:43], v[224:227]\n\t" \
"buffer_load_dwordx4 a[88:91], %[hv2], %[srd], s60 offen sc1\n\t" \
"v_mfma_f32_16x16x32_bf16 v[228:231], v[100:103], a[40:43], v[228:231]\n\t" \
"v_mfma_f32_16x16x32_bf16 v[232:235], v[104:107], a[40:43], v[232:235]\n\t" \
"v_mfma_f32_16x16x32_bf16 v[236:239], v[108:111], a[40:43], v[236:239]\n\t" \
"s_waitcnt vmcnt(11)\n\t" \
"v_mfma_f32_16x16x32_bf16 v[240:243], v[96:99], a[44:47], v[240:243]\n\t" \
"buffer_load_dwordx4 a[92:95], %[hv3], %[srd], s60 offen sc1\n\t" \
"v_mfma_f32_16x16x32_bf16 v[244:247], v[100:103], a[44:47], v[244:247]\n\t" \
"v_mfma_f32_16x16x32_bf16 v[248:251], v[104:107], a[44:47], v[248:251]\n\t" \
"v_mfma_f32_16x16x32_bf16 v[252:255], v[108:111], a[44:47], v[252:255]\n\t" \
"s_waitcnt vmcnt(11)\n\t" \
"v_mfma_f32_16x16x32_bf16 v[192:195], v[112:115], a[48:51], v[192:195]\n\t" \
"buffer_load_dwordx3 v[176:178], %[rv], %[srd], %[sR] offen offset:0\n\t" \
"v_mfma_f32_16x16x32_bf16 v[196:199], v[116:119], a[48:51], v[196:199]\n\t" \
"v_mfma_f32_16x16x32_bf16 v[200:203], v[120:123], a[48:51], v[200:203]\n\t" \
"v_mfma_f32_16x16x32_bf16 v[204:207], v[124:127], a[48:51], v[204:207]\n\t" \
"s_waitcnt vmcnt(11)\n\t" \
"v_mfma_f32_16x16x32_bf16 v[208:211], v[112:115], a[52:55], v[208:211]\n\t" \
"buffer_load_dwordx3 v[180:182], %[rv], %[srd], %[sR] offen offset:1024\n\t" \
"v_mfma_f32_16x16x32_bf16 v[212:215], v[116:119], a[52:55], v[212:215]\n\t" \
"v_mfma_f32_16x16x32_bf16 v[216:219], v[120:123], a[52:55], v[216:219]\n\t" \
"v_mfma_f32_16x16x32_bf16 v[220:223], v[124:127], a[52:55], v[220:223]\n\t" \
"s_waitcnt vmcnt(11)\n\t" \
"v_mfma_f32_16x16x32_bf16 v[224:227], v[112:115], a[56:59], v[224:227]\n\t" \
"buffer_load_dwordx3 v[184:186], %[rv], %[srd], %[sR] offen offset:256\n\t" \
"v_mfma_f32_16x16x32_bf16 v[228:231], v[116:119], a[56:59], v[228:231]\n\t" \
"v_mfma_f32_16x16x32_bf16 v[232:235], v[120:123], a[56:59], v[232:235]\n\t" \
"v_mfma_f32_16x16x32_bf16 v[236:239], v[124:127], a[56:59], v[236:239]\n\t" \
"s_waitcnt vmcnt(11)\n\t" \
"v_mfma_f32_16x16x32_bf16 v[240:243], v[112:115], a[60:63], v[240:243]\n\t" \
"buffer_load_dwordx3 v[188:190], %[rv], %[srd], %[sR] offen offset:1280\n\t" \
"v_mfma_f32_16x16x32_bf16 v[244:247], v[116:119], a[60:63], v[244:247]\n\t" \
"v_mfma_f32_16x16x32_bf16 v[248:251], v[120:123], a[60:63], v[248:251]\n\t" \
"v_mfma_f32_16x16x32_bf16 v[252:255], v[124:127], a[60:63], v[252:255]\n\t" \
"s_waitcnt vmcnt(10)\n\t" \
"v_mfma_f32_16x16x32_bf16 v[208:211], v[128:131], a[68:71], v[208:211]\n\t" \
"buffer_load_dwordx4 v[144:147], v25, %[srd], %[sGb] offen offset:0\n\t" \
"v_mfma_f32_16x16x32_bf16 v[212:215], v[132:135], a[68:71], v[212:215]\n\t" \
"v_mfma_f32_16x16x32_bf16 v[216:219], v[136:139], a[68:71], v[216:219]\n\t" \
"v_mfma_f32_16x16x32_bf16 v[220:223], v[140:143], a[68:71], v[220:223]\n\t" \
"s_waitcnt vmcnt(7)\n\t" \
"v_mfma_f32_16x16x32_bf16 v[208:211], v[160:163], a[84:87], v[208:211]\n\t" \
"buffer_load_dwordx4 v[148:151], v25, %[srd], %[sGb] offen offset:16\n\t" \
"v_mfma_f32_16x16x32_bf16 v[212:215], v[164:167], a[84:87], v[212:215]\n\t" \
"v_mfma_f32_16x16x32_bf16 v[216:219], v[168:171], a[84:87], v[216:219]\n\t" \
"v_mfma_f32_16x16x32_bf16 v[220:223], v[172:175], a[84:87], v[220:223]\n\t" \
"v_mfma_f32_16x16x32_bf16 v[224:227], v[128:131], a[72:75], v[224:227]\n\t" \
"buffer_load_dwordx4 v[152:155], v25, %[srd], %[sGb] offen offset:32\n\t" \
"v_mfma_f32_16x16x32_bf16 v[228:231], v[132:135], a[72:75], v[228:231]\n\t" \
"s_nop 0\n\t" \
"ds_write_b128 v21, v[208:211] offset:0\n\t" \
"v_mfma_f32_16x16x32_bf16 v[232:235], v[136:139], a[72:75], v[232:235]\n\t" \
"ds_write_b128 v21, v[212:215] offset:1024\n\t" \
"v_mfma_f32_16x16x32_bf16 v[236:239], v[140:143], a[72:75], v[236:239]\n\t" \
"ds_write_b128 v21, v[216:219] offset:2048\n\t" \
"s_waitcnt vmcnt(8)\n\t" \
"v_mfma_f32_16x16x32_bf16 v[224:227], v[160:163], a[88:91], v[224:227]\n\t" \
"buffer_load_dwordx4 v[156:159], v25, %[srd], %[sGb] offen offset:48\n\t" \
"v_mfma_f32_16x16x32_bf16 v[228:231], v[164:167], a[88:91], v[228:231]\n\t" \
"ds_write_b128 v21, v[220:223] offset:3072\n\t" \
"v_mfma_f32_16x16x32_bf16 v[232:235], v[168:171], a[88:91], v[232:235]\n\t" \
"v_mfma_f32_16x16x32_bf16 v[236:239], v[172:175], a[88:91], v[236:239]\n\t" \
"v_mfma_f32_16x16x32_bf16 v[240:243], v[128:131], a[76:79], v[240:243]\n\t" \
"v_mfma_f32_16x16x32_bf16 v[244:247], v[132:135], a[76:79], v[244:247]\n\t" \
"s_nop 0\n\t" \
"ds_write_b128 v22, v[224:227] offset:0\n\t" \
"v_mfma_f32_16x16x32_bf16 v[248:251], v[136:139], a[76:79], v[248:251]\n\t" \
"ds_write_b128 v22, v[228:231] offset:1024\n\t" \
"v_mfma_f32_16x16x32_bf16 v[252:255], v[140:143], a[76:79], v[252:255]\n\t" \
"ds_write_b128 v22, v[232:235] offset:2048\n\t" \
"s_waitcnt vmcnt(8)\n\t" \
"v_mfma_f32_16x16x32_bf16 v[240:243], v[160:163], a[92:95], v[240:243]\n\t" \
"v_mfma_f32_16x16x32_bf16 v[244:247], v[164:167], a[92:95], v[244:247]\n\t" \
"ds_write_b128 v22, v[236:239] offset:3072\n\t" \
"v_mfma_f32_16x16x32_bf16 v[248:251], v[168:171], a[92:95], v[248:251]\n\t" \
"v_mfma_f32_16x16x32_bf16 v[252:255], v[172:175], a[92:95], v[252:255]\n\t" \
"v_mfma_f32_16x16x32_bf16 v[192:195], v[128:131], a[64:67], v[192:195]\n\t" \
"v_mfma_f32_16x16x32_bf16 v[196:199], v[132:135], a[64:67], v[196:199]\n\t" \
"s_nop 1\n\t" \
"ds_write_b128 v23, v[240:243] offset:0\n\t" \
"v_mfma_f32_16x16x32_bf16 v[200:203], v[136:139], a[64:67], v[200:203]\n\t" \
"ds_write_b128 v23, v[244:247] offset:1024\n\t" \
"v_mfma_f32_16x16x32_bf16 v[204:207], v[140:143], a[64:67], v[204:207]\n\t" \
"ds_write_b128 v23, v[248:251] offset:2048\n\t" \
"v_mfma_f32_16x16x32_bf16 v[192:195], v[160:163], a[80:83], v[192:195]\n\t" \
"v_mfma_f32_16x16x32_bf16 v[196:199], v[164:167], a[80:83], v[196:199]\n\t" \
"ds_write_b128 v23, v[252:255] offset:3072\n\t" \
"v_mfma_f32_16x16x32_bf16 v[200:203], v[168:171], a[80:83], v[200:203]\n\t" \
"v_mfma_f32_16x16x32_bf16 v[204:207], v[172:175], a[80:83], v[204:207]\n\t" \
"s_mul_i32 s64, %[w1], 12\n\t" \
"s_add_u32 s65, %[sOut], 16384\n\t" \
"v_lshrrev_b32 v26, 3, %[lb0]\n\t" \
"v_and_b32 v27, 0x70, %[lb0]\n\t" \
"v_and_b32 v28, 0xf0, %[lb0]\n\t" \
"v_and_b32 v29, 0x60, v26\n\t" \
"v_and_b32 v26, 0x70, v26\n\t" \
"v_lshlrev_b32 v28, 3, v28\n\t" \
"v_xor_b32 v29, v29, v27\n\t" \
"v_xor_b32 v26, v26, %[lb0]\n\t" \
"v_lshrrev_b32 v24, 7, %[lb0]\n\t" \
"v_add3_u32 v28, v28, v29, s64\n\t" \
"v_add_u32 v26, s64, v26\n\t" \
"v_lshl_add_u32 v24, v24, 11, v27\n\t" \
"v_xor_b32 v29, 16, v28\n\t" \
"s_waitcnt lgkmcnt(0)\n\t" \
"s_barrier\n\t" \
"ds_read_b128 v[208:211], v20 offset:0\n\t" \
"ds_read_b128 v[212:215], v20 offset:1024\n\t" \
"ds_read_b128 v[216:219], v20 offset:2048\n\t" \
"ds_read_b128 v[220:223], v20 offset:3072\n\t" \
"ds_read_b128 v[224:227], v20 offset:4096\n\t" \
"ds_read_b128 v[228:231], v20 offset:5120\n\t" \
"ds_read_b128 v[232:235], v20 offset:6144\n\t" \
"ds_read_b128 v[236:239], v20 offset:7168\n\t" \
"ds_read_b128 v[240:243], v20 offset:8192\n\t" \
"ds_read_b128 v[244:247], v20 offset:9216\n\t" \
"ds_read_b128 v[248:251], v20 offset:10240\n\t" \
"ds_read_b128 v[252:255], v20 offset:11264\n\t" \
"s_waitcnt vmcnt(0)\n\t" \
"v_lshlrev_b32 v64, 16, v176\n\t" \
"v_and_b32 v65, 0xffff0000, v176\n\t" \
"v_lshlrev_b32 v66, 16, v180\n\t" \
"v_and_b32 v67, 0xffff0000, v180\n\t" \
"v_lshlrev_b32 v68, 16, v178\n\t" \
"v_and_b32 v69, 0xffff0000, v178\n\t" \
"v_lshlrev_b32 v70, 16, v182\n\t" \
"v_and_b32 v71, 0xffff0000, v182\n\t" \
"v_lshlrev_b32 v72, 16, v184\n\t" \
"v_and_b32 v73, 0xffff0000, v184\n\t" \
"v_lshlrev_b32 v74, 16, v188\n\t" \
"v_and_b32 v75, 0xffff0000, v188\n\t" \
"v_lshlrev_b32 v76, 16, v186\n\t" \
"v_and_b32 v77, 0xffff0000, v186\n\t" \
"v_lshlrev_b32 v78, 16, v190\n\t" \
"v_and_b32 v79, 0xffff0000, v190\n\t" \
"s_waitcnt lgkmcnt(10)\n\t" \
"v_pk_add_f32 v[192:193], v[192:193], v[208:209]\n\t" \
"v_pk_add_f32 v[194:195], v[194:195], v[210:211]\n\t" \
"v_pk_add_f32 v[196:197], v[196:197], v[212:213]\n\t" \
"v_pk_add_f32 v[198:199], v[198:199], v[214:215]\n\t" \
"s_waitcnt lgkmcnt(8)\n\t" \
"v_pk_add_f32 v[200:201], v[200:201], v[216:217]\n\t" \
"v_pk_add_f32 v[202:203], v[202:203], v[218:219]\n\t" \
"v_pk_add_f32 v[204:205], v[204:205], v[220:221]\n\t" \
"v_pk_add_f32 v[206:207], v[206:207], v[222:223]\n\t" \
"s_waitcnt lgkmcnt(6)\n\t" \
"v_pk_add_f32 v[192:193], v[192:193], v[224:225]\n\t" \
"v_pk_add_f32 v[194:195], v[194:195], v[226:227]\n\t" \
"v_pk_add_f32 v[196:197], v[196:197], v[228:229]\n\t" \
"v_pk_add_f32 v[198:199], v[198:199], v[230:231]\n\t" \
"s_waitcnt lgkmcnt(4)\n\t" \
"v_pk_add_f32 v[200:201], v[200:201], v[232:233]\n\t" \
"v_pk_add_f32 v[202:203], v[202:203], v[234:235]\n\t" \
"v_pk_add_f32 v[204:205], v[204:205], v[236:237]\n\t" \
"v_pk_add_f32 v[206:207], v[206:207], v[238:239]\n\t" \
"s_waitcnt lgkmcnt(2)\n\t" \
"v_pk_add_f32 v[192:193], v[192:193], v[240:241]\n\t" \
"v_pk_add_f32 v[194:195], v[194:195], v[242:243]\n\t" \
"v_pk_add_f32 v[196:197], v[196:197], v[244:245]\n\t" \
"v_pk_add_f32 v[198:199], v[198:199], v[246:247]\n\t" \
"s_waitcnt lgkmcnt(0)\n\t" \
"v_pk_add_f32 v[200:201], v[200:201], v[248:249]\n\t" \
"v_pk_add_f32 v[202:203], v[202:203], v[250:251]\n\t" \
"v_pk_add_f32 v[204:205], v[204:205], v[252:253]\n\t" \
"v_pk_add_f32 v[206:207], v[206:207], v[254:255]\n\t" \
"v_pk_fma_f32 v[64:65], v[192:193], v[144:145], v[64:65]\n\t" \
"v_pk_fma_f32 v[66:67], v[194:195], v[146:147], v[66:67]\n\t" \
"v_pk_fma_f32 v[68:69], v[196:197], v[148:149], v[68:69]\n\t" \
"v_pk_fma_f32 v[70:71], v[198:199], v[150:151], v[70:71]\n\t" \
"v_pk_fma_f32 v[72:73], v[200:201], v[152:153], v[72:73]\n\t" \
"v_pk_fma_f32 v[74:75], v[202:203], v[154:155], v[74:75]\n\t" \
"v_pk_fma_f32 v[76:77], v[204:205], v[156:157], v[76:77]\n\t" \
"v_pk_fma_f32 v[78:79], v[206:207], v[158:159], v[78:79]\n\t" \
"v_cvt_pk_bf16_f32 v176, v64, v65\n\t" \
"v_cvt_pk_bf16_f32 v177, v66, v67\n\t" \
"v_cvt_pk_bf16_f32 v178, v68, v69\n\t" \
"v_cvt_pk_bf16_f32 v179, v70, v71\n\t" \
"ds_write_b128 v28, v[176:179]\n\t" \
"v_cvt_pk_bf16_f32 v180, v72, v73\n\t" \
"v_cvt_pk_bf16_f32 v181, v74, v75\n\t" \
"v_cvt_pk_bf16_f32 v182, v76, v77\n\t" \
"v_cvt_pk_bf16_f32 v183, v78, v79\n\t" \
"ds_write_b128 v29, v[180:183]\n\t" \
"ds_read_b128 v[64:67], v26\n\t" \
"ds_read_b128 v[68:71], v26 offset:1024\n\t" \
"s_waitcnt lgkmcnt(1)\n\t" \
"buffer_store_dwordx4 v[64:67], v24, %[srdo], %[sOut] offen sc1\n\t" \
"s_waitcnt lgkmcnt(0)\n\t" \
"buffer_store_dwordx4 v[68:71], v24, %[srdo], s65 offen sc1\n\t" \
"s_branch Lend%=\n\t" \
"Lre%=:\n\t" \
"s_add_u32 s63, s63, 1\n\t" \
"s_cmp_ge_u32 s63, 0x40000\n\t" \
"s_cbranch_scc1 Lok%=\n\t" \
"buffer_load_dwordx4 v[128:131], %[sgv], %[srd], %[sSGl] offen offset:0 sc1\n\t" \
"buffer_load_dwordx4 v[132:135], %[sgv], %[srd], %[sSGl] offen offset:1024 sc1\n\t" \
"buffer_load_dwordx4 v[136:139], %[sgv], %[srd], %[sSGl] offen offset:2048 sc1\n\t" \
"buffer_load_dwordx4 v[140:143], %[sgv], %[srd], %[sSGl] offen offset:3072 sc1\n\t" \
"s_waitcnt vmcnt(0)\n\t" \
"s_branch Lchk%=\n\t" \
"Lps%=:\n\t" \
"buffer_load_dword v28, v26, %[srd], %[sFp] offen offset:16 sc1\n\t" \
"s_add_u32 s63, s63, 1\n\t" \
"s_cmp_ge_u32 s63, 0x40000\n\t" \
"s_waitcnt vmcnt(0)\n\t" \
"s_cbranch_scc1 Lpk%=\n\t" \
"v_cmp_ne_u32 vcc, 1, v28\n\t" \
"s_nop 1\n\t" \
"s_cbranch_vccnz Lps%=\n\t" \
"s_branch Lpk%=\n\t" \
"Lqs%=:\n\t" \
"buffer_load_dword v29, v26, %[srd], %[sFp] offen offset:0 sc1\n\t" \
"s_add_u32 s63, s63, 1\n\t" \
"s_cmp_ge_u32 s63, 0x40000\n\t" \
"s_waitcnt vmcnt(0)\n\t" \
"s_cbranch_scc1 Lqk%=\n\t" \
"v_cmp_ne_u32 vcc, 1, v29\n\t" \
"s_nop 1\n\t" \
"s_cbranch_vccnz Lqs%=\n\t" \
"s_branch Lqk%=\n\t" \
"Lend%=:\n\t"
#define K3_CLOB "v12","v13","v14","v15","v16","v17","v18","v19","v20","v21","v22","v23","v24","v25","v26","v27","v28","v29","v30","v31","v32","v33","v34","v35","v36","v37","v38","v39","v40","v41","v42","v43","v44","v45","v46","v47","v48","v49","v50","v51","v52","v53","v54","v55","v56","v57","v58","v59","v60","v61","v62","v63","v64","v65","v66","v67","v68","v69","v70","v71","v72","v73","v74","v75","v76","v77","v78","v79","v80","v81","v82","v83","v84","v85","v86","v87","v88","v89","v90","v91","v92","v93","v94","v95","v96","v97","v98","v99","v100","v101","v102","v103","v104","v105","v106","v107","v108","v109","v110","v111","v112","v113","v114","v115","v116","v117","v118","v119","v120","v121","v122","v123","v124","v125","v126","v127","v128","v129","v130","v131","v132","v133","v134","v135","v136","v137","v138","v139","v140","v141","v142","v143","v144","v145","v146","v147","v148","v149","v150","v151","v152","v153","v154","v155","v156","v157","v158","v159","v160","v161","v162","v163","v164","v165","v166","v167","v168","v169","v170","v171","v172","v173","v174","v175","v176","v177","v178","v179","v180","v181","v182","v183","v184","v185","v186","v187","v188","v189","v190","v191","v192","v193","v194","v195","v196","v197","v198","v199","v200","v201","v202","v203","v204","v205","v206","v207","v208","v209","v210","v211","v212","v213","v214","v215","v216","v217","v218","v219","v220","v221","v222","v223","v224","v225","v226","v227","v228","v229","v230","v231","v232","v233","v234","v235","v236","v237","v238","v239","v240","v241","v242","v243","v244","v245","v246","v247","v248","v249","v250","v251","v252","v253","v254","v255","a0","a1","a2","a3","a4","a5","a6","a7","a8","a9","a10","a11","a12","a13","a14","a15","a16","a17","a18","a19","a20","a21","a22","a23","a24","a25","a26","a27","a28","a29","a30","a31","a32","a33","a34","a35","a36","a37","a38","a39","a40","a41","a42","a43","a44","a45","a46","a47","a48","a49","a50","a51","a52","a53","a54","a55","a56","a57","a58","a59","a60","a61","a62","a63","a64","a65","a66","a67","a68","a69","a70","a71","a72","a73","a74","a75","a76","a77","a78","a79","a80","a81","a82","a83","a84","a85","a86","a87","a88","a89","a90","a91","a92","a93","a94","a95","a96","a97","a98","a99","a100","a101","a102","a103","a104","a105","a106","a107","a108","a109","a110","a111","a112","a113","a114","a115","a116","a117","a118","a119","a120","a121","a122","a123","a124","a125","a126","a127","a128","a129","a130","a131","a132","a133","a134","a135","a136","a137","a138","a139","a140","a141","a142","a143","a144","a145","a146","a147","a148","a149","a150","a151","a152","a153","a154","a155","a156","a157","a158","a159","a160","a161","a162","a163","a164","a165","a166","a167","a168","a169","a170","a171","a172","a173","a174","a175","a176","a177","a178","a179","a180","a181","a182","a183","a184","a185","a186","a187","a188","a189","a190","a191","a192","a193","a194","a195","a196","a197","a198","a199","a200","a201","a202","a203","a204","a205","a206","a207","a208","a209","a210","a211","a212","a213","a214","a215","a216","a217","a218","a219","a220","a221","a222","a223","a224","a225","a226","a227","a228","a229","a230","a231","a232","a233","a234","a235","a236","a237","a238","a239","a240","a241","a242","a243","a244","a245","a246","a247","a248","a249","a250","a251","a252","a253","a254","a255","s60","s61","s62","s63","s64","s65","s66","s67","s68","s69","s70","s71","s72","s73","s74","s75","s76","s77","s78","s79","s80","s81","s82","s83","s84","s85","s86","s87","s88","s89","s90","s91","s92","s93","s94","s95","vcc","scc","memory"
#define K3_XT 1
#define K3_SENT 0
#define K3_ROT 1
template <int AUX> DEV void st16(char* ws, int off, v4u v) {
  __builtin_amdgcn_raw_buffer_store_b128(v, __builtin_amdgcn_make_buffer_rsrc((void*)ws, 0, (int)WS_END, 0x00020000), off, 0, AUX);
}
template <int AUX> DEV void st16s(char* ws, int off, int soff, v4u v) {
  __builtin_amdgcn_raw_buffer_store_b128(v, __builtin_amdgcn_make_buffer_rsrc((void*)ws, 0, (int)WS_END, 0x00020000), off, soff, AUX);
}
#define AUX_XB 16
#define AUX_W2 16
#define AUX_X1 0
#define CV_ASM \
"ds_read_b128 v[48:51], %[pa] offset:0\n\t" \
"ds_read_b128 v[52:55], %[pb] offset:3584\n\t" \
"ds_read_b128 v[56:59], %[pa] offset:512\n\t" \
"ds_read_b128 v[60:63], %[pb] offset:3072\n\t" \
"ds_read_b128 v[64:67], %[pa] offset:1024\n\t" \
"ds_read_b128 v[68:71], %[pb] offset:2560\n\t" \
"ds_read_b128 v[72:75], %[pa] offset:1536\n\t" \
"ds_read_b128 v[76:79], %[pb] offset:2048\n\t" \
"s_waitcnt lgkmcnt(6)\n\t" \
"v_mfma_f32_16x16x32_bf16 v[144:147], v[48:51], v[52:55], 0\n\t" \
"ds_read_b128 v[80:83], %[pa] offset:2048\n\t" \
"v_mov_b32 v232, 0x3f803f80\n\t" \
"s_waitcnt lgkmcnt(6)\n\t" \
"v_mfma_f32_16x16x32_bf16 v[148:151], v[56:59], v[52:55], 0\n\t" \
"ds_read_b128 v[84:87], %[pb] offset:1536\n\t" \
"v_mov_b32 v233, 0x3f803f80\n\t" \
"s_waitcnt lgkmcnt(6)\n\t" \
"v_mfma_f32_16x16x32_bf16 v[148:151], v[48:51], v[60:63], v[148:151]\n\t" \
"ds_read_b128 v[88:91], %[pa] offset:2560\n\t" \
"v_mov_b32 v234, 0x3f803f80\n\t" \
"v_mfma_f32_16x16x32_bf16 v[152:155], v[56:59], v[60:63], 0\n\t" \
"ds_read_b128 v[92:95], %[pb] offset:1024\n\t" \
"v_mov_b32 v235, 0x3f803f80\n\t" \
"s_waitcnt lgkmcnt(7)\n\t" \
"v_mfma_f32_16x16x32_bf16 v[152:155], v[64:67], v[52:55], v[152:155]\n\t" \
"ds_read_b128 v[96:99], %[pa] offset:3072\n\t" \
"s_waitcnt lgkmcnt(7)\n\t" \
"v_mfma_f32_16x16x32_bf16 v[152:155], v[48:51], v[68:71], v[152:155]\n\t" \
"ds_read_b128 v[100:103], %[pb] offset:512\n\t" \
"v_mfma_f32_16x16x32_bf16 v[156:159], v[64:67], v[60:63], 0\n\t" \
"ds_read_b128 v[104:107], %[pa] offset:3584\n\t" \
"v_mfma_f32_16x16x32_bf16 v[156:159], v[56:59], v[68:71], v[156:159]\n\t" \
"ds_read_b128 v[108:111], %[pb] offset:0\n\t" \
"v_mfma_f32_16x16x32_bf16 v[160:163], v[64:67], v[68:71], 0\n\t" \
"ds_read_b64_tr_b16 v[176:177], %[pw] offset:0\n\t" \
"s_waitcnt lgkmcnt(10)\n\t" \
"v_mfma_f32_16x16x32_bf16 v[156:159], v[72:75], v[52:55], v[156:159]\n\t" \
"ds_read_b64_tr_b16 v[178:179], %[pw] offset:256\n\t" \
"s_waitcnt lgkmcnt(10)\n\t" \
"v_mfma_f32_16x16x32_bf16 v[156:159], v[48:51], v[76:79], v[156:159]\n\t" \
"ds_read_b128 v[184:187], %[pg] offset:0\n\t" \
"v_mfma_f32_16x16x32_bf16 v[160:163], v[72:75], v[60:63], v[160:163]\n\t" \
"ds_read_b128 v[188:191], %[pg] offset:16\n\t" \
"v_mfma_f32_16x16x32_bf16 v[160:163], v[56:59], v[76:79], v[160:163]\n\t" \
"ds_read_b128 v[200:203], %[ps] offset:0\n\t" \
"v_mfma_f32_16x16x32_bf16 v[164:167], v[72:75], v[68:71], 0\n\t" \
"ds_read_b64_tr_b16 v[180:181], %[pw] offset:2048\n\t" \
"v_mfma_f32_16x16x32_bf16 v[164:167], v[64:67], v[76:79], v[164:167]\n\t" \
"ds_read_b64_tr_b16 v[182:183], %[pw] offset:2304\n\t" \
"v_mfma_f32_16x16x32_bf16 v[168:171], v[72:75], v[76:79], 0\n\t" \
"s_waitcnt lgkmcnt(14)\n\t" \
"v_mfma_f32_16x16x32_bf16 v[160:163], v[80:83], v[52:55], v[160:163]\n\t" \
"ds_read_b128 v[192:195], %[pg] offset:128\n\t" \
"s_waitcnt lgkmcnt(14)\n\t" \
"v_mfma_f32_16x16x32_bf16 v[160:163], v[48:51], v[84:87], v[160:163]\n\t" \
"ds_read_b128 v[196:199], %[pg] offset:144\n\t" \
"v_mfma_f32_16x16x32_bf16 v[164:167], v[80:83], v[60:63], v[164:167]\n\t" \
"v_mfma_f32_16x16x32_bf16 v[164:167], v[56:59], v[84:87], v[164:167]\n\t" \
"v_mfma_f32_16x16x32_bf16 v[168:171], v[80:83], v[68:71], v[168:171]\n\t" \
"v_mfma_f32_16x16x32_bf16 v[168:171], v[64:67], v[84:87], v[168:171]\n\t" \
"s_waitcnt lgkmcnt(7)\n\t" \
"v_mfma_f32_16x16x32_bf16 v[172:175], v[80:83], v[76:79], 0\n\t" \
"ds_read_b128 v[204:207], %[ps] offset:64\n\t" \
"v_lshlrev_b32 v208, 16, v176\n\t" \
"v_mfma_f32_16x16x32_bf16 v[172:175], v[72:75], v[84:87], v[172:175]\n\t" \
"ds_read_b128 v[112:115], %[pa] offset:64\n\t" \
"v_and_b32 v209, 0xffff0000, v176\n\t" \
"v_mfma_f32_16x16x32_bf16 v[164:167], v[88:91], v[52:55], v[164:167]\n\t" \
"ds_read_b128 v[116:119], %[pb] offset:3648\n\t" \
"v_lshlrev_b32 v210, 16, v177\n\t" \
"v_mfma_f32_16x16x32_bf16 v[164:167], v[48:51], v[92:95], v[164:167]\n\t" \
"ds_read_b128 v[120:123], %[pb] offset:3136\n\t" \
"v_and_b32 v211, 0xffff0000, v177\n\t" \
"v_mfma_f32_16x16x32_bf16 v[168:171], v[88:91], v[60:63], v[168:171]\n\t" \
"ds_read_b128 v[124:127], %[pa] offset:576\n\t" \
"v_lshlrev_b32 v212, 16, v178\n\t" \
"v_mfma_f32_16x16x32_bf16 v[168:171], v[56:59], v[92:95], v[168:171]\n\t" \
"ds_read_b128 v[128:131], %[pb] offset:2624\n\t" \
"v_and_b32 v213, 0xffff0000, v178\n\t" \
"v_mfma_f32_16x16x32_bf16 v[172:175], v[88:91], v[68:71], v[172:175]\n\t" \
"ds_read_b128 v[132:135], %[pa] offset:1088\n\t" \
"v_lshlrev_b32 v214, 16, v179\n\t" \
"v_mfma_f32_16x16x32_bf16 v[172:175], v[64:67], v[92:95], v[172:175]\n\t" \
"ds_read_b128 v[136:139], %[pb] offset:2112\n\t" \
"v_and_b32 v215, 0xffff0000, v179\n\t" \
"s_waitcnt lgkmcnt(13)\n\t" \
"v_mfma_f32_16x16x32_bf16 v[168:171], v[96:99], v[52:55], v[168:171]\n\t" \
"ds_read_b128 v[140:143], %[pa] offset:1600\n\t" \
"v_mul_f32 v208, v208, v184\n\t" \
"v_mul_f32 v209, v209, v185\n\t" \
"v_mfma_f32_16x16x32_bf16 v[168:171], v[48:51], v[100:103], v[168:171]\n\t" \
"ds_read_b128 v[80:83], %[pb] offset:1600\n\t" \
"v_mul_f32 v210, v210, v186\n\t" \
"v_mul_f32 v211, v211, v187\n\t" \
"v_mfma_f32_16x16x32_bf16 v[172:175], v[96:99], v[60:63], v[172:175]\n\t" \
"v_mul_f32 v212, v212, v188\n\t" \
"v_mul_f32 v213, v213, v189\n\t" \
"v_mfma_f32_16x16x32_bf16 v[172:175], v[56:59], v[100:103], v[172:175]\n\t" \
"v_mul_f32 v214, v214, v190\n\t" \
"v_mul_f32 v215, v215, v191\n\t" \
"v_mfma_f32_16x16x32_bf16 v[172:175], v[104:107], v[52:55], v[172:175]\n\t" \
"v_cvt_pk_bf16_f32 v224, v208, v209\n\t" \
"v_mfma_f32_16x16x32_bf16 v[172:175], v[48:51], v[108:111], v[172:175]\n\t" \
"v_cvt_pk_bf16_f32 v225, v210, v211\n\t" \
"s_waitcnt lgkmcnt(7)\n\t" \
"v_mfma_f32_16x16x32_bf16 v[144:147], v[112:115], v[116:119], v[144:147]\n\t" \
"ds_read_b128 v[76:79], %[pa] offset:2112\n\t" \
"v_cvt_pk_bf16_f32 v226, v212, v213\n\t" \
"s_waitcnt lgkmcnt(7)\n\t" \
"v_mfma_f32_16x16x32_bf16 v[148:151], v[112:115], v[120:123], v[148:151]\n\t" \
"ds_read_b128 v[72:75], %[pb] offset:1088\n\t" \
"v_cvt_pk_bf16_f32 v227, v214, v215\n\t" \
"s_waitcnt lgkmcnt(7)\n\t" \
"v_mfma_f32_16x16x32_bf16 v[148:151], v[124:127], v[116:119], v[148:151]\n\t" \
"ds_read_b128 v[84:87], %[pa] offset:2624\n\t" \
"v_mfma_f32_16x16x32_bf16 %[T], v[200:203], v[176:179], 0\n\t" \
"s_waitcnt lgkmcnt(7)\n\t" \
"v_mfma_f32_16x16x32_bf16 v[152:155], v[112:115], v[128:131], v[152:155]\n\t" \
"ds_write_b128 %[pp], v[144:147] offset:0\n\t" \
"s_mov_b64 exec, %[em]\n\t" \
"global_store_dwordx4 %[do0], v[224:227], %[dst] sc1\n\t" \
"s_mov_b64 exec, -1\n\t" \
"v_mfma_f32_16x16x32_bf16 v[152:155], v[124:127], v[120:123], v[152:155]\n\t" \
"ds_read_b128 v[88:91], %[pb] offset:576\n\t" \
"v_mfma_f32_16x16x32_bf16 %[S], v[232:235], v[224:227], 0\n\t" \
"s_waitcnt lgkmcnt(8)\n\t" \
"v_mfma_f32_16x16x32_bf16 v[152:155], v[132:135], v[116:119], v[152:155]\n\t" \
"ds_write_b128 %[pp], v[148:151] offset:1024\n\t" \
"v_lshlrev_b32 v216, 16, v180\n\t" \
"s_waitcnt lgkmcnt(8)\n\t" \
"v_mfma_f32_16x16x32_bf16 v[156:159], v[112:115], v[136:139], v[156:159]\n\t" \
"ds_read_b128 v[68:71], %[pa] offset:3136\n\t" \
"v_and_b32 v217, 0xffff0000, v180\n\t" \
"v_mfma_f32_16x16x32_bf16 v[156:159], v[124:127], v[128:131], v[156:159]\n\t" \
"ds_read_b128 v[64:67], %[pb] offset:64\n\t" \
"v_lshlrev_b32 v218, 16, v181\n\t" \
"v_mfma_f32_16x16x32_bf16 v[156:159], v[132:135], v[120:123], v[156:159]\n\t" \
"ds_write_b128 %[pp], v[152:155] offset:2048\n\t" \
"v_and_b32 v219, 0xffff0000, v181\n\t" \
"s_waitcnt lgkmcnt(10)\n\t" \
"v_mfma_f32_16x16x32_bf16 v[156:159], v[140:143], v[116:119], v[156:159]\n\t" \
"ds_read_b128 v[92:95], %[pa] offset:3648\n\t" \
"v_lshlrev_b32 v220, 16, v182\n\t" \
"s_waitcnt lgkmcnt(10)\n\t" \
"v_mfma_f32_16x16x32_bf16 v[160:163], v[112:115], v[80:83], v[160:163]\n\t" \
"v_and_b32 v221, 0xffff0000, v182\n\t" \
"v_mfma_f32_16x16x32_bf16 v[160:163], v[124:127], v[136:139], v[160:163]\n\t" \
"v_lshlrev_b32 v222, 16, v183\n\t" \
"v_mfma_f32_16x16x32_bf16 v[160:163], v[132:135], v[128:131], v[160:163]\n\t" \
"ds_write_b128 %[pp], v[156:159] offset:3072\n\t" \
"v_and_b32 v223, 0xffff0000, v183\n\t" \
"v_mfma_f32_16x16x32_bf16 v[160:163], v[140:143], v[120:123], v[160:163]\n\t" \
"v_mul_f32 v216, v216, v192\n\t" \
"v_mul_f32 v217, v217, v193\n\t" \
"s_waitcnt lgkmcnt(10)\n\t" \
"v_mfma_f32_16x16x32_bf16 v[160:163], v[76:79], v[116:119], v[160:163]\n\t" \
"v_mul_f32 v218, v218, v194\n\t" \
"v_mul_f32 v219, v219, v195\n\t" \
"s_waitcnt lgkmcnt(9)\n\t" \
"v_mfma_f32_16x16x32_bf16 v[164:167], v[112:115], v[72:75], v[164:167]\n\t" \
"v_mul_f32 v220, v220, v196\n\t" \
"v_mul_f32 v221, v221, v197\n\t" \
"v_mfma_f32_16x16x32_bf16 v[164:167], v[124:127], v[80:83], v[164:167]\n\t" \
"v_mul_f32 v222, v222, v198\n\t" \
"v_mul_f32 v223, v223, v199\n\t" \
"v_mfma_f32_16x16x32_bf16 v[164:167], v[132:135], v[136:139], v[164:167]\n\t" \
"ds_write_b128 %[pp], v[160:163] offset:4096\n\t" \
"v_cvt_pk_bf16_f32 v228, v216, v217\n\t" \
"v_mfma_f32_16x16x32_bf16 v[164:167], v[140:143], v[128:131], v[164:167]\n\t" \
"v_cvt_pk_bf16_f32 v229, v218, v219\n\t" \
"v_mfma_f32_16x16x32_bf16 v[164:167], v[76:79], v[120:123], v[164:167]\n\t" \
"v_cvt_pk_bf16_f32 v230, v220, v221\n\t" \
"s_waitcnt lgkmcnt(9)\n\t" \
"v_mfma_f32_16x16x32_bf16 v[164:167], v[84:87], v[116:119], v[164:167]\n\t" \
"v_cvt_pk_bf16_f32 v231, v222, v223\n\t" \
"s_waitcnt lgkmcnt(7)\n\t" \
"v_mfma_f32_16x16x32_bf16 v[168:171], v[112:115], v[88:91], v[168:171]\n\t" \
"v_mfma_f32_16x16x32_bf16 %[T], v[204:207], v[180:183], %[T]\n\t" \
"v_mfma_f32_16x16x32_bf16 v[168:171], v[124:127], v[72:75], v[168:171]\n\t" \
"s_mov_b64 exec, %[em]\n\t" \
"global_store_dwordx4 %[do1], v[228:231], %[dst] sc1\n\t" \
"s_mov_b64 exec, -1\n\t" \
"v_mfma_f32_16x16x32_bf16 v[168:171], v[132:135], v[80:83], v[168:171]\n\t" \
"ds_write_b128 %[pp], v[164:167] offset:5120\n\t" \
"v_mfma_f32_16x16x32_bf16 %[S], v[232:235], v[228:231], %[S]\n\t" \
"v_mfma_f32_16x16x32_bf16 v[168:171], v[140:143], v[136:139], v[168:171]\n\t" \
"v_mfma_f32_16x16x32_bf16 v[168:171], v[76:79], v[128:131], v[168:171]\n\t" \
"v_mfma_f32_16x16x32_bf16 v[168:171], v[84:87], v[120:123], v[168:171]\n\t" \
"s_waitcnt lgkmcnt(6)\n\t" \
"v_mfma_f32_16x16x32_bf16 v[168:171], v[68:71], v[116:119], v[168:171]\n\t" \
"s_waitcnt lgkmcnt(5)\n\t" \
"v_mfma_f32_16x16x32_bf16 v[172:175], v[112:115], v[64:67], v[172:175]\n\t" \
"v_mfma_f32_16x16x32_bf16 v[172:175], v[124:127], v[88:91], v[172:175]\n\t" \
"v_mfma_f32_16x16x32_bf16 v[172:175], v[132:135], v[72:75], v[172:175]\n\t" \
"s_nop 3\n\t" \
"ds_write_b128 %[pp], v[168:171] offset:6144\n\t" \
"v_mfma_f32_16x16x32_bf16 v[172:175], v[140:143], v[80:83], v[172:175]\n\t" \
"v_mfma_f32_16x16x32_bf16 v[172:175], v[76:79], v[136:139], v[172:175]\n\t" \
"v_mfma_f32_16x16x32_bf16 v[172:175], v[84:87], v[128:131], v[172:175]\n\t" \
"v_mfma_f32_16x16x32_bf16 v[172:175], v[68:71], v[120:123], v[172:175]\n\t" \
"s_waitcnt lgkmcnt(4)\n\t" \
"v_mfma_f32_16x16x32_bf16 v[172:175], v[92:95], v[116:119], v[172:175]\n\t" \
"s_nop 7\n\t" \
"ds_write_b128 %[pp], v[172:175] offset:7168\n\t"
#define CV_CLOB "v48","v49","v50","v51","v52","v53","v54","v55","v56","v57","v58","v59","v60","v61","v62","v63","v64","v65","v66","v67","v68","v69","v70","v71","v72","v73","v74","v75","v76","v77","v78","v79","v80","v81","v82","v83","v84","v85","v86","v87","v88","v89","v90","v91","v92","v93","v94","v95","v96","v97","v98","v99","v100","v101","v102","v103","v104","v105","v106","v107","v108","v109","v110","v111","v112","v113","v114","v115","v116","v117","v118","v119","v120","v121","v122","v123","v124","v125","v126","v127","v128","v129","v130","v131","v132","v133","v134","v135","v136","v137","v138","v139","v140","v141","v142","v143","v144","v145","v146","v147","v148","v149","v150","v151","v152","v153","v154","v155","v156","v157","v158","v159","v160","v161","v162","v163","v164","v165","v166","v167","v168","v169","v170","v171","v172","v173","v174","v175","v176","v177","v178","v179","v180","v181","v182","v183","v184","v185","v186","v187","v188","v189","v190","v191","v192","v193","v194","v195","v196","v197","v198","v199","v200","v201","v202","v203","v204","v205","v206","v207","v208","v209","v210","v211","v212","v213","v214","v215","v216","v217","v218","v219","v220","v221","v222","v223","v224","v225","v226","v227","v228","v229","v230","v231","v232","v233","v234","v235","memory"
#define PFCB 64
#define SWP 1
constexpr int TROW = 132;
constexpr int KCOPY = 4160;
constexpr int UCOPY = (256 + 32) * 16 + 32;
constexpr int SA_K = 0, SA_U = 8 * KCOPY + 512, SA_W = SA_U + 4 * UCOPY, SA_G = SA_W + 16384, SA_S = SA_G + 1024, SA_R = SA_S + 512, SA_P = SA_R + 1024,
              SA_KC = SA_P + 2 * 64 * TROW * 4, SA_X = SA_KC + 2 * 1536, SA_M = SA_X + 8192 + 32, SA_J = SA_M + 4096, SA_END = SA_J + 256, SA_F = 8 * KCOPY;
#define SPINN 100000
#define WLATE 1
#define XDPP 0
#define PRIO 1
constexpr int SPIN = SPINN;
template <int CTRL> DEV u32 dppu(u32 x) { return (u32)__builtin_amdgcn_update_dpp(0, (int)x, CTRL, 0xf, 0xf, true); }
DEV u32 bfi8(u32 t, u32 v) { return (v & 0xffffff00u) | (t & 0xffu); }
DEV u32 pkc(v2f a) { return pk2(a.x, a.y); }
DEV float dot2(u32 a, float c) { return __builtin_amdgcn_fdot2_f32_bf16(__builtin_bit_cast(v2bf, a), __builtin_bit_cast(v2bf, 0x3f803f80u), c, false); }
DEV u32 p16(u32 lo, u32 hi) { return __builtin_amdgcn_perm(hi, lo, 0x05040100u); }
__global__ void __launch_bounds__(512, 2)
kA_conv(const u16* __restrict__ x, const u16* __restrict__ Are, const u16* __restrict__ Aim, const u16* __restrict__ Bre,
        const u16* __restrict__ Bim, const u16* __restrict__ Cre, const u16* __restrict__ Cim, const u16* __restrict__ logdt,
        const u16* __restrict__ Dsk, const u16* __restrict__ cvec, const u16* __restrict__ wmod, const u16* __restrict__ w2,
        const u16* __restrict__ w1, const u16* __restrict__ w3, char* __restrict__ ws) {
  __shared__ __attribute__((aligned(16))) char sm[SA_END];
  const int tid = threadIdx.x, lane = tid & 63, w = __builtin_amdgcn_readfirstlane(tid >> 6);
#define PFCM 0
#define PFCE 64
#define PFC(off) do { unsigned long long pc; int o_ = off;     \
    asm volatile("s_getpc_b64 %0\nLpc%=:\n\tv_min_u32 %1, Lcode_end-Lpc%=, %1\n\ts_mov_b32 m0, %2\n\ts_nop 0\n\tglobal_load_lds_dword %1, %0" \
                 : "=&s"(pc), "+v"(o_) : "s"(SA_J) : "memory"); } while (0)
  constexpr int NXV = (PFCM == 1);
#define PFCW 0
  if (PFCM == 2) { if ((tid >> 3) == PFCW * 8) PFC(((blockIdx.x >> 3) * 7 + (tid & 7)) * 128); }
  else if (blockIdx.x < PFCB && tid < (PFCM ? PFCE : 256)) PFC(tid * 128);
  asm volatile("" :: "s"(x), "s"(Are), "s"(Aim), "s"(Bre), "s"(Bim), "s"(Cre), "s"(Cim), "s"(logdt), "s"(Dsk), "s"(cvec), "s"(wmod), "s"(w2), "s"(w1), "s"(w3), "s"(ws));
  const int j = blockIdx.x, xc = j & 7, bi = j >> 3, P = xc * 32 + bi;
  const bool domod = j < 192;
  const int strip = j % 48, kq = j / 48;
  const bool pk = bi < 28;
  const int idx = pk ? bi >> 2 : 0, mt = bi & 1, kh = (bi >> 1) & 1;
  const int kch = (w >> 1) & 1;
  const int m = lane & 15, g = lane >> 4;
  const int sel = w >> 2;
  v4u qx0, qx1, q0, q1, q2, q3;
  u32 are = 0, aim = 0, bre = 0, bim = 0, cre = 0, cim = 0, ar2 = 0, ai2 = 0, rc = 0;
#define OP_ (P * 512 + (tid & 255) * 2)
  if (w < 4) {
    asm volatile("global_load_dwordx4 %0, %[ox], %[x]\n\tglobal_load_dwordx4 %1, %[ox], %[x] offset:512"
                 : "=&v"(qx0), "=&v"(qx1) : [x] "s"(x + j * 4096), [ox] "v"((tid >> 5) * 1024 + (tid & 31) * 16) : "memory");
    asm volatile("global_load_short_d16_hi %0, %[op], %[a0]\n\tglobal_load_short_d16_hi %1, %[op], %[a1]\n\tglobal_load_short_d16_hi %2, %[op], %[a2]\n\t"
                 "global_load_short_d16_hi %3, %[op], %[a3]\n\tglobal_load_short_d16_hi %4, %[op], %[a4]\n\tglobal_load_short_d16_hi %5, %[op], %[a5]"
                 : "+v"(are), "+v"(aim), "+v"(bre), "+v"(bim), "+v"(cre), "+v"(cim)
                 : [op] "v"(OP_), [a0] "s"(Are), [a1] "s"(Aim), [a2] "s"(Bre), [a3] "s"(Bim), [a4] "s"(Cre), [a5] "s"(Cim) : "memory");
  } else {
    asm volatile("global_load_short_d16_hi %0, %[op], %[a0]\n\tglobal_load_short_d16_hi %1, %[op], %[a1]"
                 : "+v"(ar2), "+v"(ai2) : [op] "v"(OP_), [a0] "s"(Are), [a1] "s"(Aim) : "memory");
    if (domod) {
      const int k0 = kq * 128 + (w - 4) * 32 + (lane >> 3) * 4, om = k0 * 6144 + strip * 128 + (lane & 7) * 16 + 3072;
      asm volatile("global_load_short_d16_hi %4, %[oc], %[c]\n\tglobal_load_dwordx4 %0, %[om], %[m] offset:-3072\n\tglobal_load_dwordx4 %1, %[om], %[m] offset:3072\n\t"
                   "global_load_dwordx4 %2, %[on], %[m] offset:-3072\n\tglobal_load_dwordx4 %3, %[on], %[m] offset:3072"
                   : "=&v"(q0), "=&v"(q1), "=&v"(q2), "=&v"(q3), "+v"(rc)
                   : [c] "s"(cvec), [m] "s"(wmod), [oc] "v"((k0 + (lane & 3)) * 2), [om] "v"(om), [on] "v"(om + 12288) : "memory");
    } else {
      const int o = ((((j - 192) >> 3) * 224 + (w - 4) * 8 + (lane >> 3)) * D_ + xc * 64 + (lane & 7) * 8) * 2;
#pragma unroll
      for (int i = 0; i < 7; ++i) adma(w2 + i * 32 * D_, o, SA_K + i * KCOPY + 64 + (w - 4) * 1024);
    }
    if (PFCM == 1) PFC((tid - 256 + PFCE - 8) * (j < PFCB ? 128 : 0));
  }
  u32 ep;
  asm volatile("s_load_dword %0, %1, 0x0" : "=s"(ep) : "s"(ws));
  const u32 r_dt = *(const CAS u32*)(logdt + 2 * P), r_dsk = *(const CAS u32*)(Dsk + 2 * P);
  const int fs = 2 * xc + (idx + sel > 3 && idx >= 3), tile = idx == 3 ? 12 + mt : (idx < 3 ? idx : idx - 4) * 4 + 2 * sel + mt;
  char* wdst = ws + WS_W1P + (size_t)fs * W1FS;
  const int wc = w & 1, rq = w >> 1;
  int pa = SA_K + (wc * 4 + (m & 3)) * KCOPY + (g + (m >> 2) + rq * 8) * 16;
  int pb = SA_U + (wc * 2 + (m & 1)) * UCOPY + (28 - 4 * (m >> 1) + g + rq * 8) * 16;
  const int kb = (w & 3) * 64 + 8 * g;
  int pw = SA_W + (kb + (m >> 2)) * 64 + ((m & 3) << (idx == 3 ? 3 : 4)) + (idx == 3 ? sel * 32 : sel * 8), pg = SA_G + kb * 4, ps = SA_S + kb * 2;
  int pp = SA_P + (rq * 2 + (m & 1)) * 8192 + (m >> 1) * 128 + ((g * 2 + wc) ^ ((m & 1) * 4 + ((m >> 1) & 3))) * 16;
  const int fst = tile < 8 ? 8 : (tile < 12 ? 4 : 2), fb = tile < 8 ? tile : (tile < 12 ? 120 + tile : 180 + tile);
  int do0 = lane * 16, do1 = lane * 16 + fst * 1024;
  const char* dst = wdst + ((kh * 8 + (w & 3) * 2) * fst + fb) * 1024;
  long long em = (long long)__builtin_amdgcn_readfirstlane((bi - 28) >> 31);
  int uw = SA_U + (255 - (tid >> 1)) * 16 + 8 - (tid & 1) * 8;
  int to = SA_P + kch * (64 * TROW * 4) + (tid & 127) * 4;
  int kw = SA_KC + w * 512 + m * 32 + g * 8, ka = SA_KC + sel * 1536 + (tid & 255) * 2;
  int ke = SA_K + sel * 4 * KCOPY + (tid & 255) * 16 + 48;
  int ta = SA_P + ((w >= 3) * 64 + m) * (TROW * 4) + (g & 1) * 256 + (g >> 1) * 128;
  int sx = (int)WS_XB + (P >> 1) * 32768 + (tid >> 2) * 256 + (tid & 2) * 32 + (P & 1) * 32 + (tid & 1) * 16, ss = (int)WS_ST1 + 4096 + tid * 16;
  int sa_ = (int)WS_MOD + (((m >> 2) == 3 ? 640 : (m >> 2) * 128) + (P >> 1)) * 64 + (m & 3) * 16, sb_ = (int)WS_MOD + (w < 4 ? 512 : 384) * 64 + kh * 4096 + (tid & 255) * 16;
  int ow3 = (kh * 256 + (tid >> 2)) * (F_ * 2) + (xc * 7 + idx) * 64 + (tid & 3) * 16;
  if (WLATE == 3) asm volatile("" : "+v"(ow3));
  float dt = rbf(__builtin_amdgcn_exp2f((kch ? bfhi(r_dt) : bflo(r_dt)) * 1.44269504089f));
  u32 tag;
  asm volatile("s_waitcnt lgkmcnt(0)\n\ts_add_u32 %0, %1, 0x9E3779B9\n\ts_or_b32 %0, %0, 1" : "=s"(tag) : "s"(ep) : "scc");
  u32 tb0 = tag & 0xff, tb1 = (tag >> 8) & 0xff, tb2 = (tag >> 16) & 0xff, tb3 = tag >> 24;
  asm volatile("" : "+v"(pa), "+v"(pb), "+v"(pw), "+v"(pg), "+v"(ps), "+v"(pp), "+v"(do0), "+v"(do1), "+s"(dst), "+s"(em), "+v"(uw), "+s"(tb0), "+s"(tb1), "+s"(tb2), "+s"(tb3),
               "+v"(to), "+v"(kw), "+v"(ka), "+v"(ke), "+v"(ta), "+v"(sx), "+v"(ss), "+v"(sa_), "+v"(sb_), "+v"(dt), "+s"(wdst));
  u32* t = (u32*)(sm + to);
  if (w < 4) {
    int aw = SA_X + (tid >> 5) * 1024 + (w >> 1) * 32 + (tid & 31) * 16;
    int xr = SA_X + (tid & 1) * (4096 + 32) + (tid >> 3) * 8 + (tid & 2) * 2, xw = (int)WS_XB + j * 128 + (tid >> 3) * 32768 + (tid & 7) * 16;
    u32 xm = (((tid >> 2) ^ j) & 1) ? tag : 0u, tl = tag >> ((lane >> 5) * 16), fa = SA_F, one = 1;
    int sto = (int)WS_ST1 + (w & 1) * 8192 + j * 32 + (w >> 1) * 16, fw = SA_F + w;
    asm volatile("" : "+v"(aw), "+v"(xr), "+v"(xw), "+v"(xm), "+v"(tl), "+v"(fa), "+v"(one), "+v"(sto), "+v"(fw));
    BAR();
    if (PRIO) __builtin_amdgcn_s_setprio(3);
    asm volatile("s_waitcnt vmcnt(6)\n\tds_write_b128 %2, %0\n\tds_write_b128 %2, %1 offset:512\n\tds_write_b8 %3, %4" : "+v"(qx0), "+v"(qx1) : "v"(aw), "v"(fw), "v"(one) : "memory");
    float s = dot2(qx0[0], 0.f), s2 = dot2(qx1[0], 0.f);
#pragma unroll
    for (int i = 1; i < 4; ++i) { s = dot2(qx0[i], s); s2 = dot2(qx1[i], s2); }
    s = halfsum(s + s2);
    {
      v2u a0, a1, a2, a3, b0, b1, b2, b3; u32 fl; int cnt;
      asm volatile("s_movk_i32 %[cnt], 0x7fff\n"
                   "Lxf%=:\n\tds_read_b32 %[fl], %[fa]\n\t"
                   "ds_read2st64_b32 %0, %[xr] offset1:1\n\tds_read2st64_b32 %1, %[xr] offset0:4 offset1:5\n\tds_read2st64_b32 %2, %[xr] offset0:8 offset1:9\n\tds_read2st64_b32 %3, %[xr] offset0:12 offset1:13\n\t"
                   "ds_read2st64_b32 %4, %[xr] offset0:2 offset1:3\n\tds_read2st64_b32 %5, %[xr] offset0:6 offset1:7\n\tds_read2st64_b32 %6, %[xr] offset0:10 offset1:11\n\tds_read2st64_b32 %7, %[xr] offset0:14 offset1:15\n\t"
                   "s_waitcnt lgkmcnt(8)\n\tv_cmp_ne_u32 vcc, 0x01010101, %[fl]\n\ts_sub_u32 %[cnt], %[cnt], 1\n\ts_cselect_b64 vcc, 0, vcc\n\ts_waitcnt lgkmcnt(0)\n\ts_cbranch_vccnz Lxf%="
                   : "=&v"(a0), "=&v"(a1), "=&v"(a2), "=&v"(a3), "=&v"(b0), "=&v"(b1), "=&v"(b2), "=&v"(b3), [fl] "=&v"(fl), [cnt] "=&s"(cnt), "+v"(s)
                   : [fa] "v"(fa), [xr] "v"(xr) : "memory", "vcc", "scc");
      st16s<16>(ws, xw, 0, v4u{a0.x ^ xm, a1.x ^ xm, a2.x ^ xm, a3.x ^ xm});
      st16s<16>(ws, xw, 1 << 20, v4u{a0.y ^ xm, a1.y ^ xm, a2.y ^ xm, a3.y ^ xm});
      st16s<16>(ws, xw, 2 << 20, v4u{b0.x ^ xm, b1.x ^ xm, b2.x ^ xm, b3.x ^ xm});
      st16s<16>(ws, xw, 3 << 20, v4u{b0.y ^ xm, b1.y ^ xm, b2.y ^ xm, b3.y ^ xm});
    }
    const float mu = s * (1.f / 512.f);
    v2f q = {0.f, 0.f}, qq = {0.f, 0.f};
#pragma unroll
    for (int i = 0; i < 4; ++i) {
      const v2f d0 = v2f{bflo(qx0[i]), bfhi(qx0[i])} - mu, d1 = v2f{bflo(qx1[i]), bfhi(qx1[i])} - mu;
      q = i ? __builtin_elementwise_fma(d0, d0, q) : d0 * d0; qq = i ? __builtin_elementwise_fma(d1, d1, qq) : d1 * d1;
    }
    q += qq;
    const float rs = __builtin_amdgcn_rsqf(halfsum(q.x + q.y) * (1.f / 512.f) + 1e-5f);
    const u32 gm = bfi8(tl, asu(mu)), gr = bfi8(tl >> 8, asu(rs));
    auto r0 = __builtin_amdgcn_permlane32_swap(gm, gm, false, false), r1 = __builtin_amdgcn_permlane32_swap(gr, gr, false, false);
    asm volatile("s_mov_b64 exec, 1\n\tglobal_store_dwordx4 %6, %7, %8 sc1\n\ts_mov_b64 exec, -1\n\ts_waitcnt vmcnt(5)"
                 : "+v"(are), "+v"(aim), "+v"(bre), "+v"(bim), "+v"(cre), "+v"(cim)
                 : "v"(sto), "v"(v4u{r0[0], r1[0], r0[1], r1[1]}), "s"(ws) : "memory");
    const float a = (dt * asf(are)) * 1.44269504089f, b = (dt * asf(aim)) * -0.15915494309f;
    const v2f z8 = cexpm(a, b, 8.f), z16 = cmul(z8, z8);
    v2f G0 = cmul(v2f{asf(cre), -asf(cim)}, v2f{dt * asf(bre), -(dt * asf(bim))}), G1 = cmul(G0, z8);
#pragma unroll
    for (int p = 0; p < 16; p += 2) {
      t[p * TROW] = pkc(G0); t[(p + 1) * TROW] = pkc(G1);
      if (p < 14) { G0 = cmul(G0, z16); G1 = cmul(G1, z16); }
    }
    if (PRIO) __builtin_amdgcn_s_setprio(0);
  } else {
    u32 mk[4];
#pragma unroll
    for (int i = 0; i < 4; ++i) mk[i] = ((lane >> 1) & 3) == i ? ((lane & 1) ? 0xffff0000u : 0xffffu) : 0u;
    int aw = SA_M + ((w - 4) * 2 + (lane >> 5)) * 256 + (lane & 7) * 32 + (lane & 16) + (((lane >> 3) ^ (lane >> 5)) & 1) * 2048;
    u32 tl = tag >> ((lane & 3) * 8), fa = SA_F + 4, one = 1;
    int ow = (kh * 256 + ((tid & 255) >> 2)) * (F_ * 2) + (xc * 7 + idx) * 64 + (tid & 3) * 16;
    const u16* wp = mt ? w3 : w1;
    int mo = (int)WS_MOD + strip * 1024 + kq * 16 + lane * 16, mr = SA_M + lane * 4;
    asm volatile("" : "+v"(mk[0]), "+v"(mk[1]), "+v"(mk[2]), "+v"(mk[3]), "+v"(aw), "+v"(tl), "+v"(fa), "+v"(one), "+v"(ow), "+v"(mo), "+v"(mr));
    *(v4u*)(sm + (w < 6 ? SA_U + (w - 4) * 2 * UCOPY + 4080 : 0) + (lane >> (w < 6 ? 5 : 2)) * (w < 6 ? UCOPY : KCOPY) + (lane & (w < 6 ? 31 : 3)) * 16) = v4u{0u, 0u, 0u, 0u};
    BAR();
    asm volatile("s_waitcnt vmcnt(%2)" : "+v"(ar2), "+v"(ai2) : "n"(5 + NXV));
#define WDMA() _Pragma("unroll") for (int i = 0; i < 4; ++i) adma(wp + i * 64 * F_, ow, SA_W + i * 4096 + (w - 4) * 1024)
    if (!WLATE) WDMA();
    const float a = (dt * asf(ar2)) * 1.44269504089f, b = (dt * asf(ai2)) * 0.15915494309f;
    v2f wv[8], Z[9];
    wv[1] = cexpm(a, b, 1.f); Z[1] = cexpm(a, b, 128.f); Z[8] = cexpm(a, b, 1024.f);
    wv[2] = cmul(wv[1], wv[1]); Z[2] = cmul(Z[1], Z[1]); wv[3] = cmul(wv[2], wv[1]); Z[3] = cmul(Z[2], Z[1]); wv[4] = cmul(wv[2], wv[2]); Z[4] = cmul(Z[2], Z[2]);
#pragma unroll
    for (int i = 5; i < 8; ++i) { wv[i] = cmul(wv[4], wv[i - 4]); Z[i] = cmul(Z[4], Z[i - 4]); }
    u32* e = t + 16 * TROW;
    e[0] = pk2(1.f, 0.f);
#pragma unroll
    for (int q = 1; q < 9; ++q) e[q * TROW] = pkc(Z[q]);
#pragma unroll
    for (int q = 1; q < 9; ++q) e[(8 + q) * TROW] = pkc(cmul(Z[8], Z[q]));
#pragma unroll
    for (int r = 1; r < 8; r += 2) { e[(16 + r) * TROW] = pkc(wv[r]); e[(17 + r) * TROW] = pkc(cmul(Z[1], wv[r])); }
    e[32 * TROW] = pkc(wv[4]); e[40 * TROW] = pkc(wv[2]); e[44 * TROW] = pkc(wv[6]);
#pragma unroll
    for (int q = 1; q < 8; ++q) e[(32 + q) * TROW] = pkc(cmul(Z[q], wv[4]));
#pragma unroll
    for (int q = 1; q < 4; ++q) { e[(40 + q) * TROW] = pkc(cmul(Z[q], wv[2])); e[(44 + q) * TROW] = pkc(cmul(Z[q], wv[6])); }
    if (domod) {
      asm volatile("s_waitcnt vmcnt(%1)" : "+v"(rc) : "n"(4 + NXV + (WLATE ? 0 : 4)));
      const float cf_ = asf(rc), sv = cf_ * __builtin_amdgcn_rcpf(1.f + __builtin_amdgcn_exp2f(-1.44269504089f * cf_));
      const u32 s2 = pk2(sv, sv);
      const u32 s0 = dppu<0x00>(s2), s1 = dppu<0x55>(s2), s2b = dppu<0xAA>(s2), s3 = dppu<0xFF>(s2);
      v4u a0 = {s0 & mk[0], s0 & mk[1], s0 & mk[2], s0 & mk[3]}, a1 = {s1 & mk[0], s1 & mk[1], s1 & mk[2], s1 & mk[3]};
      v4u a2 = {s2b & mk[0], s2b & mk[1], s2b & mk[2], s2b & mk[3]}, a3 = {s3 & mk[0], s3 & mk[1], s3 & mk[2], s3 & mk[3]};
      asm volatile("s_waitcnt vmcnt(%8)" : "+v"(q0), "+v"(q1), "+v"(q2), "+v"(q3), "+v"(a0), "+v"(a1), "+v"(a2), "+v"(a3) : "n"(NXV + (WLATE ? 0 : 4)));
      v4f acc = mfma16(a0, q0, v4f{0.f, 0.f, 0.f, 0.f});
      acc = mfma16(a1, q1, acc); acc = mfma16(a2, q2, acc); acc = mfma16(a3, q3, acc);
      *(v4f*)(sm + aw) = acc;
      u32 old; v2f p0, p1, p2, p3;
      asm volatile("s_mov_b64 exec, 1\n\tds_add_rtn_u32 %0, %5, %6\n\ts_mov_b64 exec, -1\n\t"
                   "ds_read2st64_b32 %1, %7 offset1:1\n\tds_read2st64_b32 %2, %7 offset0:2 offset1:3\n\tds_read2st64_b32 %3, %7 offset0:4 offset1:5\n\tds_read2st64_b32 %4, %7 offset0:6 offset1:7\n\t"
                   "s_waitcnt lgkmcnt(0)" : "=&v"(old), "=&v"(p0), "=&v"(p1), "=&v"(p2), "=&v"(p3) : "v"(fa), "v"(one), "v"(mr) : "memory");
      if (__builtin_amdgcn_readfirstlane(old) == 3) {
        const v2f pq = (p0 + p1) + (p2 + p3);
        const u32 tt = bfi8(tl, asu(pq.x + pq.y));
        asm volatile("s_mov_b64 exec, %3\n\tglobal_store_dwordx4 %0, %1, %2 sc1\n\ts_mov_b64 exec, -1\n\ts_nop 0"
                     :: "v"(mo), "v"(v4u{tt, dppu<0x55>(tt), dppu<0xAA>(tt), dppu<0xFF>(tt)}), "s"(ws), "s"(0x1111111111111111ull) : "memory");
      }
    } else asm volatile("s_waitcnt vmcnt(%0)" :: "n"(NXV + (WLATE ? 0 : 4)) : "memory");
#define WSK 1
    if (WLATE == 1 && (pk || !WSK)) WDMA();
  }
  v4u x0, x1, s0, s1, ma, mb;
#define SWEEP(W, C, CX, CS, CM) asm volatile("global_load_dwordx4 %0, %[sx], %[ws]" CX "\n\tglobal_load_dwordx4 %1, %[sx], %[ws] offset:128" CX "\n\t" \
                 "global_load_dwordx4 %2, %[ss], %[ws] offset:-4096" CS "\n\tglobal_load_dwordx4 %3, %[st], %[ws]" CS "\n\t" \
                 "global_load_dwordx4 %4, %[sa], %[ws]" CM "\n\tglobal_load_dwordx4 %5, %[sb], %[ws]" CM W \
                 : C(x0), C(x1), C(s0), C(s1), C(ma), C(mb) \
                 : [ws] "s"(ws), [sx] "v"(sx), [ss] "v"(ss), [st] "v"(ss + 4096), [sa] "v"(sa_), [sb] "v"(sb_) : "memory")
#define SWX " sc1"
#define SWS " sc1"
#define SWM " sc1"
  BAR();
  if (SWP == 0) SWEEP("", "=&v", SWX, SWS, SWM);
  if (w < 6) {
    v4u A[8], B[8];
    const int tbo = (16 + 16 * (w - 3 * (w >= 3))) * (TROW * 4);
#pragma unroll
    for (int s = 0; s < 8; ++s) { A[s] = *(const v4u*)(sm + ta + 16 * s); B[s] = *(const v4u*)(sm + ta + tbo + 16 * s); }
    SCHED();
    v4f acc = {0.f, 0.f, 0.f, 0.f};
    acc[0] = lane == 0 ? (w == 0 ? bflo(r_dsk) : (w == 3 ? bfhi(r_dsk) : 0.f)) : 0.f;
#pragma unroll
    for (int s = 0; s < 8; ++s) acc = mfma16(A[s], B[s], acc);
    *(v2u*)(sm + kw) = v2u{pk2(acc[0], acc[1]), pk2(acc[2], acc[3])};
  } else {
    if (w == 6) *(v4u*)(ws + WS_ST2 + (size_t)j * 1024 + lane * 16) = v4u{~0u, ~0u, ~0u, ~0u};
    else if (j < 8) *(v4u*)(ws + WS_FLAG + j * 1024 + lane * 16) = v4u{0u, 0u, 0u, 0u};
    if (!domod) {
      const int kr = (j - 192) >> 3;
      const char* src = sm + SA_K + 64 + (8 * g + (m >> 2)) * 128 + (m & 3) * 32 + (w - 6) * 16;
#pragma unroll
      for (int i = 0; i < 7; ++i) {
        int pos = i == 3 ? 12 + (kr & 1) : (i & 3) * 4 + (kr & 1) * 2 + (i >> 2);
        if (pos < 8) pos = (pos - xc) & 7;
        else { pos -= (xc * 3) >> 2; if (pos < 8) pos += 6; }
#pragma unroll
        for (int h = 0; h < 2; ++h) {
          const char* s = src + i * KCOPY + h * 8;
          v2u a0 = trread(s), a1 = trread(s + 512);
          st16<AUX_W2>(ws, (int)WS_W2P + (((xc * 4 + (kr >> 1)) * 14 + pos) * 4 + (w - 6) * 2 + h) * 1024 + lane * 16, v4u{a0.x, a0.y, a1.x, a1.y});
        }
      }
    }
  }
  BAR();
  if (SWP == 1) SWEEP("", "=&v", SWX, SWS, SWM);
  if (WLATE == 3) { const u16* wp = mt ? w3 : w1; adma(wp, ow3, SA_W + w * 1024); adma(wp + 128 * F_, ow3, SA_W + 8192 + w * 1024); }
  {
    const u16* kp = (const u16*)(sm + ka);
    u32 o[4];
    const u32 e0 = kp[0], en = kp[1];
    if (w & 2) {
      o[0] = o[1] = p16(e0, e0); o[2] = p16((lane & 1) ? en : e0, en); o[3] = p16(en, en);
    } else {
      const u32 e4 = kp[512];
      if (w & 1) {
        o[0] = p16(e0, e0); o[1] = p16(e0, e4); o[2] = p16(e4, e4); o[3] = p16(en, en);
      } else {
        const u32 e1 = kp[272], e3 = kp[304], e5 = kp[336], e7 = kp[368], e2 = kp[640], e6 = kp[704];
        const bool lo = lane < 32;
        o[0] = p16(e0, lo ? e1 : e0); o[1] = p16(e2, lo ? e3 : e4); o[2] = p16(e4, lo ? e5 : e4); o[3] = p16(e6, lo ? e7 : en);
      }
    }
    char* d = sm + ke;
    *(v4u*)(d + 3 * KCOPY) = v4u{o[0], o[1], o[2], o[3]};
    *(v2u*)(d + KCOPY + 8) = v2u{o[0], o[1]}; *(v2u*)(d + KCOPY + 16) = v2u{o[2], o[3]};
    *(u32*)(d + 12) = o[0]; *(v2u*)(d + 16) = v2u{o[1], o[2]}; *(u32*)(d + 24) = o[3];
    *(u32*)(d + 2 * KCOPY + 4) = o[0]; *(v2u*)(d + 2 * KCOPY + 8) = v2u{o[1], o[2]}; *(u32*)(d + 2 * KCOPY + 16) = o[3];
  }
  if (SWP == 2) SWEEP("", "=&v", SWX, SWS, SWM);
  const v4u tb4 = {tb0, tb1, tb2, tb3};
  v4u c0, c1, ca, cb;
  if (WLATE == 3) asm volatile("s_waitcnt vmcnt(2)" : "+v"(x0), "+v"(x1), "+v"(s0), "+v"(s1), "+v"(ma), "+v"(mb) :: "memory");
  else asm volatile("s_waitcnt vmcnt(0)" : "+v"(x0), "+v"(x1), "+v"(s0), "+v"(s1), "+v"(ma), "+v"(mb) :: "memory");
#pragma unroll 1
  for (int spin = 0;; ++spin) {
    c0 = s0 ^ tb4; c1 = s1 ^ tb4; ca = ma ^ tb4; cb = mb ^ tb4;
    const v4u o4 = (c0 | c1) | (ca | cb);
    u32 bad = ((o4.x | o4.y) | (o4.z | o4.w)) & 0xff;
#pragma unroll
    for (int i = 0; i < 4; ++i) {
      u32 y0 = x0[i] ^ dppu<0x4E>(x0[i]), y1 = x1[i] ^ dppu<0x4E>(x1[i]);
      asm("" : "+v"(y0), "+v"(y1));
      bad |= y0 ^ tag; bad |= y1 ^ tag;
    }
    if (__builtin_expect(!__any(bad != 0) || spin == SPIN, 1)) {
      break;
    }
    if (bad) SWEEP("\n\ts_waitcnt vmcnt(0)", "+v", " sc1", " sc1", " sc1");
  }
  const v4u xv = (tid & 2) ? x1 : x0;
  float ga0, ga1; u32 pvs;
  {
    float v0 = asf((P & 1) ? ca.z : ca.x), v1 = asf((P & 1) ? ca.w : ca.y);
    v0 += dppf<0xB1>(v0); v1 += dppf<0xB1>(v1); v0 += dppf<0x4E>(v0); v1 += dppf<0x4E>(v1);
    const u32 pv = pk2(v0, v1), pg1 = pk2(1.f + bflo(pv), 1.f + bfhi(pv));
    const u32 sa = __builtin_amdgcn_readlane(pv, 0), g1 = __builtin_amdgcn_readlane(pg1, 4), ga = __builtin_amdgcn_readlane(pv, 8);
    ga0 = bflo(ga); ga1 = bfhi(ga);
    pvs = pv;
    const float mu[4] = {asf(c0.x), asf(c0.z), asf(c1.x), asf(c1.z)}, rs[4] = {asf(c0.y), asf(c0.w), asf(c1.y), asf(c1.w)};
#pragma unroll
    for (int ch = 0; ch < 2; ++ch) {
      float h[4];
#pragma unroll
      for (int i = 0; i < 4; ++i) h[i] = ((ch ? bfhi(xv[i]) : bflo(xv[i])) - mu[i]) * rs[i] * (ch ? bfhi(g1) : bflo(g1)) + (ch ? bfhi(sa) : bflo(sa));
      char* u0 = sm + uw + ch * 2 * UCOPY;
      const u32 p32 = pk2(h[3], h[2]), p10 = pk2(h[1], h[0]);
      *(v2u*)(u0 + UCOPY) = v2u{p32, p10};
      *(u16*)(u0 - 2) = (u16)p32;
      *(u32*)u0 = __builtin_amdgcn_alignbit(p10, p32, 16);
      *(u16*)(u0 + 4) = (u16)(p10 >> 16);
    }
    const bool o1 = lane & 1, o2 = lane & 2;
    float t0 = asf(o1 ? cb.y : cb.x) + dppf<0xB1>(asf(o1 ? cb.x : cb.y)), t1 = asf(o1 ? cb.w : cb.z) + dppf<0xB1>(asf(o1 ? cb.z : cb.w));
    const float mm = rbf((o2 ? t1 : t0) + dppf<0x4E>(o2 ? t0 : t1));
    if (w < 4) *(float*)(sm + SA_G + tid * 4) = rbf(1.f + mm);
    else *(u16*)(sm + SA_S + (tid - 256) * 2) = (u16)(asu(mm) >> 16);
  }
  if (WLATE == 3) WAITV(0);
  BAR();
#define PFW 3
  if (PFW && w < PFW)
    asm volatile("s_mov_b32 m0, %2\n\ts_nop 0\n\tglobal_load_lds_dword %1, %0" :: "s"(ws + WS_W2P + bi * 57344 + (7 - PFW) * 8192), "v"(tid * 128), "s"(SA_J) : "memory");
  if (tid == 460) *(v2f*)(ws + WS_GB + P * 8) = v2f{bflo(pvs), bfhi(pvs)};
  if (j == 0 && tid == 0) *(u32*)(ws + WS_EP) = tag;
  {
    v4f S, T;
    asm volatile(CV_ASM : [S] "=&v"(S), [T] "=&v"(T)
                 : [pa] "v"(pa), [pb] "v"(pb), [pw] "v"(pw), [pg] "v"(pg), [ps] "v"(ps), [pp] "v"(pp), [do0] "v"(do0), [do1] "v"(do1), [dst] "s"(dst), [em] "s"(em) : CV_CLOB);
    if (lane < 16) *(v2f*)(sm + SA_R + (sel * 16 + lane) * 32 + (w & 3) * 8) = v2f{S[0], T[0]};
  }
  BAR();
  {
    const int ch = tid & 1;
    const char* p0 = sm + SA_P + (tid ^ ((tid >> 3) & 3)) * 16;
    const char* p1 = sm + SA_P + 8192 + (tid ^ ((tid >> 3) & 3) ^ 4) * 16;
    const v4f e0 = (*(const v4f*)p0 + *(const v4f*)(p0 + 16384)) + (*(const v4f*)(p0 + 32768) + *(const v4f*)(p0 + 49152));
    const v4f e1 = (*(const v4f*)p1 + *(const v4f*)(p1 + 16384)) + (*(const v4f*)(p1 + 32768) + *(const v4f*)(p1 + 49152));
    const float r[8] = {e0[0], e1[0], e0[1], e1[1], e0[2], e1[2], e0[3], e1[3]};
    v4u o;
#pragma unroll
    for (int i = 0; i < 4; ++i) {
      const float give = ch ? r[i] : r[4 + i], keep = ch ? r[4 + i] : r[i];
      const float got = dppf<0xB1>(give);
      o[i] = pk2(bflo(xv[i]) + ga0 * (ch ? got : keep), bfhi(xv[i]) + ga1 * (ch ? keep : got));
    }
    if (XDPP) {
      const bool hi = lane & 2;
      const u32 g0 = dppu<0x4E>(hi ? o[0] : o[2]), g1 = dppu<0x4E>(hi ? o[1] : o[3]);
      const int xd = (int)WS_X1 + (P >> 4) * 131072 + (P & 1) * 1024 + ((P >> 2) & 3) * 256 + (P & 2) * 4 + w * 16384 + (lane >> 3) * 2048 + (lane & 4) * 32 + (lane & 1) * 32 + (lane & 2) * 8;
      __builtin_amdgcn_raw_buffer_store_b64(hi ? v2u{g0, g1} : v2u{o[0], o[1]}, __builtin_amdgcn_make_buffer_rsrc((void*)ws, 0, (int)WS_END, 0x00020000), xd, 0, AUX_X1);
      __builtin_amdgcn_raw_buffer_store_b64(hi ? v2u{o[2], o[3]} : v2u{g0, g1}, __builtin_amdgcn_make_buffer_rsrc((void*)ws, 0, (int)WS_END, 0x00020000), xd + 64, 0, AUX_X1);
    } else {
    char* xt = sm + SA_U + w * 1024;
    *(v4u*)(xt + lane * 16) = o;
    const int xd = (int)WS_X1 + (P >> 4) * 131072 + (P & 1) * 1024 + ((P >> 2) & 3) * 256 + (P & 2) * 4 + w * 16384 + (lane >> 4) * 2048 + (lane & 15) * 16;
#pragma unroll
    for (int i = 0; i < 2; ++i)
      __builtin_amdgcn_raw_buffer_store_b64(*(const v2u*)(xt + i * 512 + lane * 8), __builtin_amdgcn_make_buffer_rsrc((void*)ws, 0, (int)WS_END, 0x00020000), xd, i * 8192, AUX_X1);
    }
    if ((w & 3) == 0 && lane < 16 && pk) {
      const v4f p = *(const v4f*)(sm + SA_R + (sel * 16 + lane) * 32), q = *(const v4f*)(sm + SA_R + (sel * 16 + lane) * 32 + 16);
      const float s = (p[0] + p[2]) + (q[0] + q[2]), t = (p[1] + p[3]) + (q[1] + q[3]);
      const float sh = rbf(s), th = rbf(t);
      *(v4u*)(wdst + 16 * 14 * 1024 + tile * 512 + kh * 256 + lane * 16) = v4u{pk2(sh, sh), pk2(s - sh, th), pk2(th, t - th), 0u};
    }
  }
}

constexpr int SM3_END = 5 * 28672 + 8192;
__global__ void __launch_bounds__(256, 1)
k3_mlp(char* __restrict__ ws, u16* __restrict__ out) {
  const int lane = threadIdx.x & 63, w = __builtin_amdgcn_readfirstlane(threadIdx.x >> 6);
  const int b = blockIdx.x, xcd = b & 7, bi = b >> 3;
  const int rb = (xcd & 1) * 8 + (bi & 7), fs = (xcd >> 1) * 4 + (bi >> 3);
  const int t2 = xcd * 4 + (bi & 3), ct = bi >> 2;
  const int c = lane & 15, g = lane >> 4;
  __amdgpu_buffer_rsrc_t rws = __builtin_amdgcn_make_buffer_rsrc((void*)ws, 0, (int)WS_END, 0x00020000);
  __amdgpu_buffer_rsrc_t rout = __builtin_amdgcn_make_buffer_rsrc((void*)out, 0, L_ * D_ * 2, 0x00020000);
  const int sX = (int)WS_X1 + (rb * 8 + w * 2) * 1024, sH = (int)WS_H2 + (fs >> 2) * 14 * 131072 + (rb * 8 + w * 2) * 1024,
            sHl = (int)WS_H2 + w * 14 * 131072 + t2 * 4096, wb = (int)WS_W1P + fs * W1FS;
  asm volatile(K3_ASM "Lcode_end:"
    :
    : [lb0] "v"(lane * 16), [lb1] "v"(lane * 16 + 57344), [lb2] "v"(lane * 16 + 114688), [sgv] "v"(g * 4096 + c * 16),
      [tv] "v"((g & 1) * 1024 + (g >> 1) * 256 + c * 16), [rv] "v"((g >> 1) * 131072 + (g & 1) * 512 + c * 16),
      [hv0] "v"(lane * 16 + w * 1024), [hv1] "v"(lane * 16 + ((w + 1) & 3) * 1024), [hv2] "v"(lane * 16 + ((w + 2) & 3) * 1024), [hv3] "v"(lane * 16 + ((w + 3) & 3) * 1024),
      [srd] "s"(rws), [srdo] "s"(rout),
      [sX] "s"(sX), [sXs] "s"(sX + fs * 131072), [sW7] "s"(wb + w * 7168), [w7] "s"(w * 7168), [sW1] "s"(wb + w * 1024), [w1] "s"(w * 1024),
      [sW2] "s"((int)WS_W2P + (ct * 56 + w * 14) * 4096), [sSGl] "s"((int)WS_ST2 + rb * 16384 + w * 256), [sSGs] "s"((int)WS_ST2 + ((rb * 16 + fs) * 4 + w) * 256),
      [sH2s] "s"(sH + (fs & 3) * 131072), [sH2t] "s"(sH + (12 + ((fs >> 1) & 1)) * 131072 + (fs & 1) * 512),
      [sFs] "s"((int)WS_FLAG + (rb * 4 + (fs >> 2)) * 128 + (fs & 3) * 4), [sFp] "s"((int)WS_FLAG + ((t2 >> 1) * 4 + w) * 128),
      [sHl] "s"(sHl), [sHr] "s"(sHl + ct * 131072), [sHr2] "s"(sHl + (8 + ((ct * 3) >> 2)) * 131072), [sR] "s"((int)WS_X1 + ct * 2 * 131072 + t2 * 4096 + (K3_XT ? (w >> 1) * 2048 + (w & 1) * 4 : w * 1024)),
      [sGb] "s"((int)WS_GB + ct * 256), [sOut] "s"((t2 * 64 + (K3_XT ? (w >> 1) * 32 + (w & 1) : 16 * w)) * 1024 + ct * 128)
    : K3_CLOB);
}

extern "C" void kernel_launch(void* const* d_in, const int* in_sizes, int n_in, void* d_out, int out_size, void* d_ws,
                              size_t ws_size, hipStream_t stream) {
  const u16* const* in = (const u16* const*)d_in;
  char* ws = (char*)d_ws;
  (void)hipFuncSetAttribute((const void*)k3_mlp, hipFuncAttributeMaxDynamicSharedMemorySize, SM3_END);
  kA_conv<<<256, 512, 0, stream>>>(in[0], in[1], in[2], in[3], in[4], in[5], in[6], in[7], in[8], in[13], in[14], in[12], in[10], in[11], ws);
  k3_mlp<<<256, 256, SM3_END, stream>>>(ws, (u16*)d_out);
}
```
